```python
import torch

def _softmax(x, axis=-1):
    return torch.softmax(x, dim=axis)
def _gelu(x):
    return torch.nn.functional.gelu(x, approximate="tanh")
def _silu(x):
    return torch.nn.functional.silu(x)
def _sigmoid(x):
    return torch.sigmoid(x)
def _layernorm(x, axis=-1, eps=1e-5):
    m = x.mean(dim=axis, keepdim=True)
    v = x.var(dim=axis, keepdim=True, unbiased=False)
    return (x - m) / torch.sqrt(v + eps)
def _scatter_add(tgt, idx, src, axis=0):
    return tgt.clone().index_add_(axis, idx, src)
def _segment_sum(src, seg, n):
    out = torch.zeros((n,) + src.shape[1:], dtype=src.dtype)
    return out.index_add_(0, seg, src)
def _pad(x, pw):
    flat = tuple(v for pair in reversed(pw) for v in pair)
    return torch.nn.functional.pad(x, flat)

kernel_name = 'standalone__gqa_block_cached__near_ridge'

def setup_inputs():

    _w = (lambda a: (lambda t: t.to(torch.bfloat16) if t.is_floating_point() else t)(torch.as_tensor(a)))
    B, Sq, D = 8, 96, 512
    Hq, Hkv, Sk, Dh = 32, 32, 1024, 128
    assert Hq % Hkv == 0, (Hq, Hkv)
    return {
        "x": _w((lambda *s: torch.randn(*s, dtype=torch.bfloat16) * 0.8)(B, Sq, D)),
        "k_cache": _w((lambda *s: torch.randn(*s, dtype=torch.bfloat16) * 0.8)(B, Hkv, Sk, Dh)),
        "v_cache": _w((lambda *s: torch.randn(*s, dtype=torch.bfloat16) * 0.05)(B, Hkv, Sk, Dh)),
        "wq": _w((lambda *s: torch.randn(*s, dtype=torch.bfloat16) * 0.8)(D, Hq * Dh) * (D ** -0.5)),
        "wo": _w((lambda *s: torch.randn(*s, dtype=torch.bfloat16) * 0.05)(Hq * Dh, D) * ((Hq * Dh) ** -0.5)),
    }

def reference(x, k_cache, v_cache, wq, wo):
    B, Sq, D = x.shape
    Hq, Dh = 32, 128
    Hkv = k_cache.shape[1]
    rep = Hq // Hkv
    q = (lambda x, *d: x.permute(*d))((x @ wq).reshape(B, Sq, Hq, Dh), 0, 2, 1, 3)
    k = (lambda x, n, axis: torch.repeat_interleave(x, n, dim=axis))(k_cache, rep, axis=1)
    v = (lambda x, n, axis: torch.repeat_interleave(x, n, dim=axis))(v_cache, rep, axis=1)
    scores = torch.einsum("bhqd,bhkd->bhqk", q, k) * (Dh ** -0.5)
    p = _softmax(scores, axis=-1)
    o = torch.einsum("bhqk,bhkd->bhqd", p, v)
    return (lambda x, *d: x.permute(*d))(o, 0, 2, 1, 3).reshape(B, Sq, Hq * Dh) @ wo
```

```cpp
#define TAU 56
#include <hip/hip_runtime.h>
#include <hip/hip_bf16.h>
#include <stdint.h>
#include <type_traits>

#define LAS __attribute__((address_space(3)))
#define GAS __attribute__((address_space(1)))
#define DI __device__ __forceinline__
typedef __attribute__((ext_vector_type(4))) __bf16 bf16x4;
typedef __attribute__((ext_vector_type(8))) __bf16 bf16x8;
typedef __attribute__((ext_vector_type(2))) __bf16 bf16x2;
typedef __attribute__((ext_vector_type(4))) float f32x4;
typedef __attribute__((ext_vector_type(16))) float f32x16;
typedef __attribute__((ext_vector_type(2))) float f32x2;
typedef __attribute__((ext_vector_type(4))) unsigned u32x4;
typedef __attribute__((ext_vector_type(2))) unsigned u32x2;

constexpr int SQ = 96, DM = 512, NH = 32, SK = 1024, DH = 128;
constexpr int SLOT = 16384;
constexpr int KRING = 0, VRING = 4 * SLOT, EXTRA = 8 * SLOT;
constexpr int CTRL = EXTRA + 28672;
constexpr int C_WX = CTRL, C_KV = CTRL + 8, C_WL = CTRL + 16, C_WL2 = CTRL + 24, C_DONE = CTRL + 32, C_QRDY = CTRL + 64, C_MRG = CTRL + 96,
              C_RED = CTRL + 128, C_CLR = CTRL + 160, C_PEERS = CTRL + 192, C_OFL = CTRL + 200, C_OCNT = CTRL + 208, C_OISS = CTRL + 212,
              C_DUMMY = CTRL + 256;
constexpr int LDS_BYTES = 163840;
constexpr float SCL = 0.08838834764831845f * 1.4426950408889634f;
constexpr float PSUM_MAX = 4096.0f;
constexpr unsigned WBUF = 0x65840658u, XBUF = 0x73921739u;
constexpr unsigned OSLOT = 0x76210543u;
constexpr unsigned WS_O = 0, WS_P = 8u << 20, WS_H = 15u << 20, WS_F = WS_H + (64u << 10), WS_G = WS_F + (256u << 10), WS_BYTES = 16u << 20;
constexpr int SPIN_MAX = 1 << 16;

DI unsigned cvtpk(float lo, float hi) { f32x2 v = {lo, hi}; bf16x2 b = __builtin_convertvector(v, bf16x2); return __builtin_bit_cast(unsigned, b); }
DI f32x4 mfma16(bf16x8 a, bf16x8 b, f32x4 c) { return __builtin_amdgcn_mfma_f32_16x16x32_bf16(a, b, c, 0, 0, 0); }
DI f32x16 mfma32(bf16x8 a, bf16x8 b, f32x16 c) { return __builtin_amdgcn_mfma_f32_32x32x16_bf16(a, b, c, 0, 0, 0); }
DI bf16x4 trread(LAS char* p) { return __builtin_amdgcn_ds_read_tr16_b64_v4bf16((LAS bf16x4*)p); }
DI bf16x8 cat8(bf16x4 a, bf16x4 b) { return __builtin_shufflevector(a, b, 0, 1, 2, 3, 4, 5, 6, 7); }
DI bf16x8 asbf(const u32x4& w) { return __builtin_bit_cast(bf16x8, w); }
DI float xmax2(float v) {
  auto s = __builtin_amdgcn_permlane32_swap(__float_as_uint(v), __float_as_uint(v), false, false);
  return fmaxf(__uint_as_float(s[0]), __uint_as_float(s[1]));
}
DI u32x4 exp8(const f32x16& s, int o) {
  u32x4 w;
#pragma unroll
  for (int i = 0; i < 4; ++i) w[i] = cvtpk(__builtin_amdgcn_exp2f(s[o + 2 * i]), __builtin_amdgcn_exp2f(s[o + 2 * i + 1]));
  return w;
}
#define CFENCE() asm volatile("" ::: "memory")
#define LGKM0() do { CFENCE(); __builtin_amdgcn_s_waitcnt(0xc07f); CFENCE(); } while (0)
#define VMW(N) asm volatile("s_waitcnt vmcnt(" #N ")" ::: "memory")
#define SBAR() __builtin_amdgcn_sched_barrier(0)
#define PIN(x) asm volatile("" : "+v"(x))
DI int rfl(int v) { return __builtin_amdgcn_readfirstlane(v); }
DI int lds_word(LAS char* p) { return rfl(*(volatile LAS int*)p); }
DI int lds_min2(LAS char* p) { u32x2 v = *(volatile LAS u32x2*)p; return rfl(min((int)v[0], (int)v[1])); }
DI int lds_min6(LAS char* p) {
  u32x4 v = *(volatile LAS u32x4*)p; u32x2 w = *(volatile LAS u32x2*)(p + 16);
  return rfl(min(min(min((int)v[0], (int)v[1]), min((int)v[2], (int)v[3])), min((int)w[0], (int)w[1])));
}
DI LAS char* post_addr(LAS char* lds, int off, int lane) { return lds + (lane == 0 ? off : C_DUMMY + 4 * lane); }
DI void lds_post(LAS char* lds, int off, int v, int lane) { *(volatile LAS int*)post_addr(lds, off, lane) = v; }
DI void lds_bump(LAS char* p) { __hip_atomic_fetch_add((LAS int*)p, 1, __ATOMIC_RELAXED, __HIP_MEMORY_SCOPE_WORKGROUP); }
typedef __amdgpu_buffer_rsrc_t rsrc_t;
DI void st16(rsrc_t r, unsigned off, u32x4 v) { __builtin_amdgcn_raw_buffer_store_b128(v, r, (int)off, 0, 16); }
DI u32x4 ld16(rsrc_t r, unsigned voff, unsigned soff) { return __builtin_amdgcn_raw_buffer_load_b128(r, (int)voff, (int)soff, 16); }
DI void st4(rsrc_t r, unsigned off, unsigned v) { __builtin_amdgcn_raw_buffer_store_b32(v, r, (int)off, 0, 16); }
DI unsigned ld4(rsrc_t r, unsigned voff, unsigned soff) { return __builtin_amdgcn_raw_buffer_load_b32(r, (int)voff, (int)soff, 16); }
DI int fresh_lane() { int l; asm volatile("v_mbcnt_lo_u32_b32 %0, -1, 0\n\tv_mbcnt_hi_u32_b32 %0, -1, %0" : "=v"(l)); return l; }
DI int unit_head(int pos, int h8, int b) { int n = pos - 1 + 2 * (b & 3); n = n >= 7 ? n - 7 : n; return pos == 0 ? h8 : ((h8 + 1 + n) & 7); }

#define STAMP(i) do {} while (0)

DI int spin_gt(LAS char* p, int n) {
  int v, r;
  asm volatile("1:\n\tds_read_b32 %1, %2\n\ts_waitcnt lgkmcnt(0)\n\tv_readfirstlane_b32 %0, %1\n\ts_cmp_le_i32 %0, %3\n\ts_cbranch_scc1 1b"
               : "=&s"(r), "=&v"(v) : "v"(p), "s"(n) : "memory", "scc");
  return r;
}
DI void spin_all_gt(LAS char* p, int n) {
  int v;
  asm volatile("1:\n\tds_read_b32 %0, %1\n\ts_waitcnt lgkmcnt(0)\n\tv_cmp_ge_i32 vcc, %2, %0\n\ts_cbranch_vccnz 1b"
               : "=&v"(v) : "v"(p), "v"(n) : "memory", "vcc");
}
DI int spin_bit(LAS char* p, int bit) {
  int v, r;
  asm volatile("1:\n\tds_read_b32 %1, %2\n\ts_waitcnt lgkmcnt(0)\n\tv_readfirstlane_b32 %0, %1\n\ts_bitcmp0_b32 %0, %3\n\ts_cbranch_scc1 1b"
               : "=&s"(r), "=&v"(v) : "v"(p), "s"(bit) : "memory", "scc");
  return r;
}
DI void wait_gt(int& have, int n, LAS char* p) {
  while (have <= n) { have = lds_word(p); if (have <= n) __builtin_amdgcn_s_sleep(1); }
  CFENCE();
}

struct Attn {
  f32x16 o[4];
  f32x16 negm;
  bf16x8 qf[8], kf[8];
  bf16x4 vf[16];
  u32x4 pf[2];
  bf16x8 ones;
  float lsum;
  int have;
  LAS char *kb[8], *vb[4], *land, *donep;

  DI void release() { LGKM0(); lds_bump(donep); }
  template <int S> DI void kread(int t) { kf[t] = *(LAS bf16x8*)(kb[t] + S * SLOT); }
  template <int S> DI void vread(int i) { vf[i] = trread(vb[(i >> 1) & 3] + S * SLOT + (i >> 3) * 4096 + (i & 1) * 2048); }
  DI bf16x8 vfrag(int m) { return cat8(vf[2 * m], vf[2 * m + 1]); }
  DI float rowmax(const f32x16& s) {
    float mx = s[0];
#pragma unroll
    for (int r = 1; r < 16; ++r) mx = fmaxf(mx, s[r]);
    return xmax2(mx);
  }
  DI f32x4 psum(const u32x4& p0, const u32x4& p1) { return mfma16(ones, asbf(p1), mfma16(ones, asbf(p0), f32x4{0.f, 0.f, 0.f, 0.f})); }
  template <int S> DI void tile(f32x16& cur, f32x16& nxt, int i) {
    release(); wait_gt(have, 2 * i + 2, land);
    SBAR();
    f32x4 ts = {0.f, 0.f, 0.f, 0.f};
    float ea, eb, pa = 0.f, pb = 0.f;
#pragma unroll
    for (int m = 0; m < 8; ++m) {
      nxt = mfma32(kf[m], qf[m], m == 0 ? negm : nxt);
      if (m < 5) { vread<S>(3 * m); vread<S>(3 * m + 1); vread<S>(3 * m + 2); }
      if (m == 5) vread<S>(15);
      ea = __builtin_amdgcn_exp2f(cur[2 * m]); eb = __builtin_amdgcn_exp2f(cur[2 * m + 1]);
      asm volatile("" : "+v"(ea), "+v"(eb));
      if (m > 0) { unsigned w = cvtpk(pa, pb); PIN(w); pf[(m - 1) >> 2][(m - 1) & 3] = w; }
      pa = ea; pb = eb;
      if (m == 6) ts = mfma16(ones, asbf(pf[0]), ts);
      SBAR();
    }
    pf[1][3] = cvtpk(pa, pb);
    ts = mfma16(ones, asbf(pf[1]), ts);
    release(); wait_gt(have, 2 * i + 3, land);
    if (__builtin_expect(__any(!(ts[0] <= PSUM_MAX)), 0)) {
      const float dl = fmaxf(rowmax(cur), 0.f);
      const float al = __builtin_amdgcn_exp2f(-dl);
#pragma unroll
      for (int r = 0; r < 16; ++r) { cur[r] -= dl; nxt[r] -= dl; negm[r] -= dl; }
      PIN(negm);
      pf[0] = exp8(cur, 0); pf[1] = exp8(cur, 8);
      ts = psum(pf[0], pf[1]);
      lsum *= al;
#pragma unroll
      for (int c = 0; c < 4; ++c) o[c] *= al;
      PIN(lsum); PIN(ts); PIN(nxt); PIN(pf[0]); PIN(pf[1]);
    }
    PIN(lsum); PIN(ts);
    lsum += ts[0];
    SBAR();
#pragma unroll
    for (int m = 0; m < 8; ++m) {
      o[m & 3] = mfma32(vfrag(m), asbf(pf[m >> 2]), o[m & 3]);
      if (m < 4) { kread<(S + 2) & 3>(2 * m); kread<(S + 2) & 3>(2 * m + 1); }
      SBAR();
    }
  }
};

__global__ __launch_bounds__(512, 2) void fused_kernel(const __bf16* __restrict__ x, const __bf16* __restrict__ kc, const __bf16* __restrict__ vc,
                                                       const __bf16* __restrict__ wq, const __bf16* __restrict__ wo, __bf16* __restrict__ out, char* ws) {
  extern __shared__ __attribute__((aligned(16))) char smem[];
  LAS char* lds = (LAS char*)smem;
  const int tid = threadIdx.x, lane = tid & 63, wave = (0x54321076u >> (4 * rfl(tid >> 6))) & 15;
  const int bid = blockIdx.x, xcd = bid & 7, idx = bid >> 3;
  const int oct = xcd & 3, h8 = idx & 7;
  const int h = oct * 8 + h8, b = (xcd >> 2) * 4 + (idx >> 3);
  const int wg = b * NH + h;
  using T = std::true_type; using F = std::false_type;
  STAMP(0);

  if (__builtin_expect(wave >= 6, 1)) {
    __builtin_amdgcn_s_setprio(3);
    const int L = wave - 6;
    const int r16 = lane & 15, g = lane >> 4;
    const char* wp = (const char*)(wq + h * DH) + (size_t)L * 32 * 8192;
    const char* xp = (const char*)(x + ((size_t)b * SQ + 48 * L) * DM);
    unsigned oa[4], ob[4];
#pragma unroll
    for (int i = 0; i < 4; ++i) {
      oa[i] = i * (32768 - 1024) + g * 8192 + ((r16 ^ (g << 2)) << 4);
      const int row = 8 * i + (lane >> 3);
      ob[i] = row * 1024 - i * 1024 + (((lane & 7) ^ ((row >> 1) & 7)) << 4);
    }
    LAS char* lb = lds + L * 8192;
    int seen = 0;
#define GLDS(src, dst, off, aux) __builtin_amdgcn_global_load_lds((const GAS void*)(src), (LAS void*)(dst), 16, off, aux)
#define GLDS4(src, o0, o1, o2, o3, dst, aux) do { GLDS((src) + (o0), dst, 0, aux); GLDS((src) + (o1), dst, 1024, aux); GLDS((src) + (o2), dst, 2048, aux); GLDS((src) + (o3), dst, 3072, aux); } while (0)
#define POST(p, v) (*(volatile LAS int*)(p) = (v))
    auto wait_done = [&](int need) {
      while (seen < need) { seen = lds_min6(lds + C_DONE); if (seen < need) __builtin_amdgcn_s_sleep(1); }
      CFENCE();
    };
    LAS char* pw = post_addr(lds, C_WX + 4 * L, lane);
#pragma unroll 1
    for (int cc = 0; cc < 8; ++cc) {
      wait_done(cc - 4);
      LAS char* xd = lds + ((XBUF >> (4 * cc)) & 15) * SLOT + L * 6144;
      LAS char* wd = lb + ((WBUF >> (4 * cc)) & 15) * SLOT;
      const char* xs = xp + cc * 128;
      GLDS4(xs, ob[0], ob[1], ob[2], ob[3], xd, 0);
      GLDS(xs + 32768 + ob[0], xd + 4096, 0, 0); GLDS(xs + 32768 + ob[1], xd + 4096, 1024, 0);
      if (cc >= 2) { VMW(20); POST(pw, cc - 1); }
      const char* wsrc = wp + (size_t)cc * 64 * 8192;
      GLDS4(wsrc, oa[0], oa[1], oa[2], oa[3], wd, 0);
      GLDS4(wsrc + 16 * 8192, oa[0], oa[1], oa[2], oa[3], wd + 4096, 0);
      STAMP(1 + cc);
      if (cc == 0) asm volatile("s_barrier" ::: "memory");
    }
    const char* kp = (const char*)(kc + (size_t)wg * SK * DH) + L * 8192;
    const char* vp = (const char*)(vc + (size_t)wg * SK * DH) + L * 8192;
    const unsigned ov = g * 256 + ((r16 ^ (g << 2)) << 4);
#pragma unroll
    for (int i = 0; i < 4; ++i) {
      oa[i] = g * 256 + ((r16 ^ (4 * i + g)) << 4);
      const int row = 8 * i + (lane >> 3);
      ob[i] = row * 1024 - i * 1024 + (((lane & 7) ^ (((row >> 1) & 1) << 2)) << 4);
    }
    auto issue_k = [&](int n) {
      const char* src = kp + n * SLOT; LAS char* dst = lb + KRING + (n & 3) * SLOT;
      GLDS4(src, oa[0], oa[1], oa[2], oa[3], dst, 2); GLDS4(src + 4096, oa[0], oa[1], oa[2], oa[3], dst + 4096, 2);
    };
    auto issue_v = [&](int n) {
      const char* src = vp + n * SLOT; LAS char* dst = lb + VRING + (n & 3) * SLOT;
      GLDS4(src, ov, ov, ov, ov, dst, 2); GLDS4(src + 4096, ov, ov, ov, ov, dst + 4096, 2);
    };
    wait_done(4);
    unsigned due = (unsigned)__builtin_amdgcn_s_memrealtime() + 2 * TAU;
    issue_k(0);
    STAMP(17);
    VMW(22); POST(pw, 7);
    issue_k(1);
    VMW(16); POST(pw, 8);
    LAS char* pk = post_addr(lds, C_KV + 4 * L, lane);
    auto gate = [&](int j, int need) {
      if (seen < need) {
        seen = lds_min6(lds + C_DONE);
        if (seen < need) {
          VMW(0); POST(pk, j);
          do { __builtin_amdgcn_s_sleep(1); seen = lds_min6(lds + C_DONE); } while (seen < need);
        }
      }
      while ((int)((unsigned)__builtin_amdgcn_s_memrealtime() - due) < 0) __builtin_amdgcn_s_sleep(1);
      due += TAU;
      CFENCE();
    };
#define PUBLISH(j) do { VMW(8); POST(pk, j); } while (0)
#pragma unroll 1
    for (int n = 0; n < 12; ++n) {
      const int j = 2 * n + 2;
      gate(j, n < 4 ? ((0x9875 >> (4 * n)) & 15) : j + 1);
      issue_v(n); PUBLISH(j);
      gate(j + 1, n < 3 ? ((0x975 >> (4 * n)) & 15) : j + 2);
      issue_k(n + 2); PUBLISH(j + 1);
      STAMP(18 + n);
    }
    gate(26, 28); issue_k(14); PUBLISH(26);
    gate(27, 30); issue_k(15); PUBLISH(27);
    gate(28, 30); issue_v(12); PUBLISH(28);
    gate(29, 30); issue_v(13); PUBLISH(29);
    gate(30, 31); issue_v(14); PUBLISH(30);
    gate(31, 33); issue_v(15);
    STAMP(48);
    PUBLISH(31);
    const char* op = (const char*)(wo + (size_t)(oct * 1024 + 64 * L) * DM + h8 * 64);
    auto issue_o = [&](int pos) {
      const char* src = op + (size_t)unit_head(pos, h8, b) * 128 * 1024; LAS char* dst = lb + ((OSLOT >> (4 * pos)) & 15) * SLOT;
      GLDS4(src, ob[0], ob[1], ob[2], ob[3], dst, 0); GLDS4(src + 32768, ob[0], ob[1], ob[2], ob[3], dst + 4096, 0);
    };
    LAS char* pl = post_addr(lds, C_WL + 4 * L, lane);
    __builtin_amdgcn_s_setprio(0);
    wait_done(36);
    issue_o(0);
    PUBLISH(32);
    STAMP(49);
    wait_done(37); issue_o(1);
    while (lds_word(lds + C_OISS) < 6) __builtin_amdgcn_s_sleep(1);
    wait_done(41);
    issue_o(2);
    VMW(8); POST(pl, 2);
    if (L == 1) {
      LAS char* pl2 = post_addr(lds, C_WL2, lane);
#pragma unroll 1
      for (int pos = 3; pos < 8; ++pos) {
        issue_o(pos);
        op -= 65536; lb -= 8192; issue_o(pos); op += 65536; lb += 8192;
        VMW(16); POST(pos == 3 ? pl : pl2, pos);
      }
      VMW(0); POST(pl2, 8);
    } else {
      VMW(0); POST(pl, 3);
      const rsrc_t rs = __builtin_amdgcn_make_buffer_rsrc(ws, 0, WS_BYTES, 0x00020000);
      if (lds_word(lds + C_PEERS) == 0) {
        const bool peer = lane < 32 && ((lane >> 3) == oct || (lane & 7) == h8);
        const unsigned hello_to = peer ? WS_H + (unsigned)(b * NH + lane) * 128 + h * 4 : WS_BYTES;
        for (int spin = 0; spin < SPIN_MAX; ++spin) {
          st4(rs, hello_to, 1u); __builtin_amdgcn_s_sleep(8);
          if (!__any(peer && ld4(rs, (lane & 31) * 4, WS_H + (unsigned)wg * 128) != 1u)) break;
        }
        st4(rs, hello_to, 1u);
      }
      while (lds_word(lds + C_OCNT) < 6) __builtin_amdgcn_s_sleep(2);
      LAS char* pm = post_addr(lds, C_OFL, lane);
      for (int spin = 0; spin < SPIN_MAX; ++spin) {
        CFENCE();
        const unsigned m = (unsigned)__builtin_amdgcn_ballot_w64(ld4(rs, (lane & 7) * 128, WS_F + (unsigned)(b * NH + oct * 8) * 128) == 1u) & 0xffu;
        POST(pm, spin == SPIN_MAX - 1 ? 0xff : (int)m);
        if (m == 0xffu) break;
        __builtin_amdgcn_s_sleep(2);
      }
    }
    STAMP(58);
    return;
  }
  const int kh = wave >= 3, rb = wave < 3 ? wave : (wave == 3 ? 2 : wave - 4);
  const int partner = rb == 2 ? (wave ^ 1) : (wave ^ 4);
  const int widx = 2 * rb + kh;
  if (wave == 0) *(LAS int*)(lds + CTRL + 4 * lane) = 0;
  asm volatile("s_waitcnt lgkmcnt(0)\n\ts_barrier" ::: "memory");
  const int r32 = lane & 31, hh = lane >> 5, blk = (lane >> 4) & 1, qq = (lane & 15) >> 2, p4 = lane & 3;
  const rsrc_t rs = __builtin_amdgcn_make_buffer_rsrc(ws, 0, WS_BYTES, 0x00020000);
  const unsigned o_out = WS_O + (unsigned)((wg * 3 + rb) * 8 + 4 * kh) * 1024;
  const unsigned p_in = WS_P + (unsigned)(wg * 24 + widx) * 1024;
  Attn st;
  st.donep = post_addr(lds, C_DONE + 4 * wave, lane);
  {
    f32x16 qa[2] = {f32x16{}, f32x16{}};
    const int wadr = (8 * hh + qq) * 256 + (((4 * kh + blk) ^ (qq << 1)) << 5) + p4 * 8;
    const int xrow = 32 * rb + r32;
    const int xadr = xrow * 128 + ((hh ^ ((xrow >> 1) & 7)) << 4);
    bf16x8 xa[4], xb[4];
    bf16x4 wa[16], wb[16];
    LAS char* wxp = lds + C_WX + 4 * (lane & 1);
    auto qp_step = [&](auto MMA, auto RD, bf16x8 (&xc)[4], bf16x4 (&wc)[16], int cn, bf16x8 (&xn)[4], bf16x4 (&wn)[16]) {
      LAS char* xb_ = lds + ((XBUF >> (4 * cn)) & 15) * SLOT;
      LAS char* wb_ = lds + ((WBUF >> (4 * cn)) & 15) * SLOT;
      int fl = 0;
#pragma unroll
      for (int m = 0; m < 8; ++m) {
        if constexpr (MMA.value) qa[m & 1] = mfma32(cat8(wc[2 * m], wc[2 * m + 1]), xc[m >> 1], qa[m & 1]);
        if constexpr (RD.value) {
          constexpr int lo[7] = {0, 4, 8, 11, 14, 17, 20};
          if (m < 6) {
#pragma unroll
            for (int i = lo[m]; i < lo[m + 1]; ++i) {
              const int ks = i / 5, e = i % 5 - 1;
              if (e < 0) xn[ks] = *(LAS bf16x8*)(xb_ + (xadr ^ (ks << 5)));
              else wn[4 * ks + e] = trread(wb_ + (wadr ^ ((e >> 1) << 6)) + ks * 4096 + (e & 1) * 1024);
            }
          }
          if (m == 5) fl = *(volatile LAS int*)wxp;
        }
        SBAR();
      }
      if constexpr (RD.value) {
        st.release();
        if (cn < 7 && __any(fl <= cn + 1)) spin_all_gt(wxp, cn + 1);
        CFENCE();
      }
    };
    while (lds_min2(lds + C_WX) < 1) __builtin_amdgcn_s_sleep(1);
    CFENCE();
    STAMP(1);
    qp_step(F{}, T{}, xb, wb, 0, xa, wa);
#pragma unroll 1
    for (int cc = 0; cc < 6; cc += 2) {
      qp_step(T{}, T{}, xa, wa, cc + 1, xb, wb);
      qp_step(T{}, T{}, xb, wb, cc + 2, xa, wa);
    }
    STAMP(8);
    qp_step(T{}, T{}, xa, wa, 7, xb, wb);
    qp_step(T{}, F{}, xb, wb, 0, xa, wa);
    spin_all_gt(lds + C_DONE + 4 * min(lane, 5), 5);
    LAS char* ql = lds + EXTRA + xrow * 256;
#pragma unroll
    for (int i = 0; i < 2; ++i)
#pragma unroll
      for (int r4 = 0; r4 < 4; ++r4) {
        const int ch = 4 * (2 * kh + i) + r4;
        u32x2 w = {cvtpk(qa[i][4 * r4] * SCL, qa[i][4 * r4 + 1] * SCL), cvtpk(qa[i][4 * r4 + 2] * SCL, qa[i][4 * r4 + 3] * SCL)};
        *(LAS u32x2*)(ql + ((ch ^ (r32 & 15)) << 4) + hh * 8) = w;
      }
    LGKM0();
    lds_post(lds, C_QRDY + 4 * wave, 1, lane);
    spin_all_gt(lds + (lane ? C_KV + 4 * kh : C_QRDY + 4 * partner), lane ? 1 : 0);
#pragma unroll
    for (int t = 0; t < 8; ++t) st.qf[t] = *(LAS bf16x8*)(ql + (((2 * t + hh) ^ (r32 & 15)) << 4));
  }
  STAMP(9);
  {
    const int kadr = KRING + kh * 8192 + r32 * 256 + ((hh ^ (r32 & 15)) << 4);
    const int vadr = VRING + kh * 8192 + (4 * hh + qq) * 256 + ((blk ^ (qq << 1)) << 5) + p4 * 8;
#pragma unroll
    for (int t = 0; t < 8; ++t) st.kb[t] = lds + (kadr ^ (t << 5));
#pragma unroll
    for (int c = 0; c < 4; ++c) st.vb[c] = lds + (vadr ^ ((c ^ (2 * kh)) << 6));
    const unsigned one = (blk == (qq & 1)) ? 0x3f803f80u : 0u;
    st.ones = asbf(u32x4{one, one, one, one});
  }
  st.land = lds + C_KV + 4 * kh;
#pragma unroll
  for (int c = 0; c < 4; ++c) st.o[c] = f32x16{};
  st.lsum = 0.f; st.have = 0;
  f32x16 sA, sB;
  {
#pragma unroll
    for (int t = 0; t < 8; ++t) st.kread<0>(t);
    SBAR();
    sA = f32x16{};
#pragma unroll
    for (int t = 0; t < 8; ++t) sA = mfma32(st.kf[t], st.qf[t], sA);
    SBAR();
    st.release();
#pragma unroll
    for (int t = 0; t < 8; ++t) st.kread<1>(t);
    SBAR();
    const float mx = st.rowmax(sA);
#pragma unroll
    for (int r = 0; r < 16; ++r) { sA[r] -= mx; st.negm[r] = -mx; }
  }
  auto hello = [&]() {
    const int ln = fresh_lane();
    const bool peer = ln < 32 && ((ln >> 3) == oct || (ln & 7) == h8);
    st4(rs, peer ? WS_H + (unsigned)(b * NH + ln) * 128 + h * 4 : WS_BYTES, 1u);
  };
  STAMP(10);
#pragma unroll 1
  for (int it = 0; it < 12; it += 4) {
    st.tile<0>(sA, sB, it); st.tile<1>(sB, sA, it + 1); st.tile<2>(sA, sB, it + 2); st.tile<3>(sB, sA, it + 3);
    if (wave == 2) {
      if (it == 0) {
        const int ln = fresh_lane();
        st16(rs, ln < 48 ? WS_G + (unsigned)wg * 1024 + ln * 16 : (ln < 56 ? WS_F : WS_H) + (unsigned)wg * 128 + (ln & 7) * 16, u32x4{0u, 0u, 0u, 0u});
      } else if (it == 4) {
        VMW(0);
        hello();
      }
    }
    STAMP(11 + (it >> 2));
  }
  const int ln_ = fresh_lane();
  const int r32_ = ln_ & 31, hh_ = ln_ >> 5, blk_ = (ln_ >> 4) & 1, qq_ = (ln_ & 15) >> 2, p4_ = ln_ & 3;
  u32x4 of[8][4];
  {
    unsigned hin = 0;
    if (wave == 2) hin = ld4(rs, (ln_ & 31) * 4, WS_H + (unsigned)wg * 128);
    u32x4 pq[8];
    auto sm_step = [&](auto GG, f32x16& cur, f32x16& nxt) {
      constexpr int G = GG.value;
      if constexpr (G < 3) st.release();
      SBAR();
#pragma unroll
      for (int t = 0; t < 8; ++t) {
        if constexpr (G < 3) nxt = mfma32(st.kf[t], st.qf[t], t == 0 ? st.negm : nxt);
        unsigned w = cvtpk(__builtin_amdgcn_exp2f(cur[2 * t]), __builtin_amdgcn_exp2f(cur[2 * t + 1]));
        PIN(w); pq[2 * G + (t >> 2)][t & 3] = w;
        SBAR();
      }
      f32x4 ts = st.psum(pq[2 * G], pq[2 * G + 1]);
      if constexpr (G < 2) {
        wait_gt(st.have, 26 + G, st.land);
#pragma unroll
        for (int t = 0; t < 8; ++t) st.kread<2 + G>(t);
      }
      SBAR();
      if (__builtin_expect(__any(!(ts[0] <= PSUM_MAX)), 0)) {
        const float dl = __builtin_ceilf(fmaxf(st.rowmax(cur), 0.f));
        const float al = __builtin_amdgcn_exp2f(-dl);
#pragma unroll
        for (int r = 0; r < 16; ++r) { cur[r] -= dl; if (G < 3) nxt[r] -= dl; st.negm[r] -= dl; }
        PIN(st.negm);
        pq[2 * G] = exp8(cur, 0); pq[2 * G + 1] = exp8(cur, 8);
        ts = st.psum(pq[2 * G], pq[2 * G + 1]);
        st.lsum *= al;
#pragma unroll
        for (int c = 0; c < 4; ++c) st.o[c] *= al;
#pragma unroll
        for (int i = 0; i < 8 * G; ++i) { const unsigned w = pq[i >> 2][i & 3]; pq[i >> 2][i & 3] = cvtpk(__uint_as_float(w << 16) * al, __uint_as_float(w & 0xffff0000u) * al); }
        PIN(st.lsum); PIN(ts); if (G < 3) PIN(nxt);
#pragma unroll
        for (int i = 0; i < 2 * G + 2; ++i) PIN(pq[i]);
      }
      PIN(st.lsum); PIN(ts);
      st.lsum += ts[0];
    };
    sm_step(std::integral_constant<int, 0>{}, sA, sB);
    sm_step(std::integral_constant<int, 1>{}, sB, sA);
    if (wave == 2) {
      if (!__any(ln_ < 32 && ((ln_ >> 3) == oct || (ln_ & 7) == h8) && hin != 1u)) { hello(); lds_post(lds, C_PEERS, 1, ln_); }
    }
    sm_step(std::integral_constant<int, 2>{}, sA, sB);
    sm_step(std::integral_constant<int, 3>{}, sB, sA);
    const float lsum = st.lsum, mrun = -st.negm[0];
    {
      LAS char* xp_ = lds + EXTRA + widx * 4096 + ln_ * 16;
#pragma unroll
      for (int i = 0; i < 4; ++i) *(LAS u32x4*)(xp_ + i * 1024) = pq[4 + i];
      *(LAS f32x2*)(lds + EXTRA + 24576 + widx * 512 + ln_ * 8) = f32x2{mrun, lsum};
    }
    LGKM0();
    lds_post(lds, C_MRG + 4 * wave, 1, ln_);
    STAMP(39);
    wait_gt(st.have, 28, st.land);
#pragma unroll
    for (int j = 0; j < 16; ++j) st.vread<0>(j);
    SBAR();
#pragma unroll
    for (int m = 0; m < 8; ++m) st.o[m & 3] = mfma32(st.vfrag(m), asbf(pq[m >> 2]), st.o[m & 3]);
    st.release();
    SBAR();
    LAS char* mrgp = lds + C_MRG + 4 * partner;
    spin_all_gt(ln_ < 6 ? lds + C_DONE + 4 * ln_ : mrgp, ln_ < 6 ? 34 : 0);
    u32x4 pp[4];
    const f32x2 ml = *(LAS f32x2*)(lds + EXTRA + 24576 + (widx ^ 1) * 512 + ln_ * 8);
    {
      LAS char* pp_ = lds + EXTRA + (widx ^ 1) * 4096 + ln_ * 16;
#pragma unroll
      for (int i = 0; i < 4; ++i) pp[i] = *(LAS u32x4*)(pp_ + i * 1024);
    }
    if (st.have <= 29) spin_gt(st.land, 29);
#pragma unroll
    for (int j = 0; j < 16; ++j) st.vread<1>(j ^ 4);
    const float mn = fmaxf(mrun, ml[0]);
    const float a0 = __builtin_amdgcn_exp2f(mrun - mn), a1 = __builtin_amdgcn_exp2f(ml[0] - mn);
    const float inv = 1.0f / (a0 * lsum + a1 * ml[1]);
    float f0 = a0 * inv, f1 = a1 * inv;
    PIN(f0); PIN(f1);
    SBAR();
    LAS char* xo = lds + KRING + widx * 8192 + ln_ * 16;
#pragma unroll
    for (int m = 0; m < 8; ++m) {
      const int mm = m < 2 ? m + 2 : (m < 4 ? m + 4 : (m < 6 ? m - 4 : m - 2));
      st.o[mm & 3] = mfma32(st.vfrag(mm), asbf(pq[2 + (mm >> 2)]), st.o[mm & 3]);
      if (m >= 5) {
#pragma unroll
        for (int q = (m - 5) * 3; q < (m - 5) * 3 + 3 && q < 8; ++q) {
          const f32x16& v = st.o[2 + (q >> 2)]; const int r = 4 * (q & 3);
          *(LAS f32x4*)(xo + q * 1024) = f32x4{v[r], v[r + 1], v[r + 2], v[r + 3]};
        }
      }
      SBAR();
    }
    st.release();
    lds_post(lds, C_MRG + 4 * wave, 2, ln_);
    STAMP(42);
    st.vb[2] = st.vb[0] + (kh ? -8192 : 8192); st.vb[3] = st.vb[1] + (kh ? -8192 : 8192);
    LAS char* kvp = lds + C_KV + 4 * (ln_ & 1);
    LAS char* po = lds + KRING + (widx ^ 1) * 8192 + ln_ * 16;
    auto get_o = [&]() {
#pragma unroll
      for (int q = 0; q < 8; ++q) {
        const f32x4 t = *(volatile LAS f32x4*)(po + q * 1024);
#pragma unroll
        for (int r = 0; r < 4; ++r) st.o[2 + (q >> 2)][4 * (q & 3) + r] = t[r];
      }
    };
    spin_all_gt(kvp, 30);
#pragma unroll
    for (int j = 0; j < 16; ++j) st.vread<2>((j & 3) + ((j >> 2) & 1) * 8 + (j >> 3) * 4);
    const int fl = *(volatile LAS int*)mrgp;
    get_o();
    SBAR();
#pragma unroll
    for (int q = 0; q < 4; ++q) { const int m = (q & 1) + (q >> 1) * 4; st.o[m & 3] = mfma32(st.vfrag(m), asbf(pq[4 + (m >> 2)]), st.o[m & 3]); }
    SBAR();
    if (rfl(fl) < 2) { spin_gt(mrgp, 1); get_o(); }
    st.release(); lds_bump(st.donep);
#pragma unroll
    for (int q = 0; q < 4; ++q) { const int m = 2 + (q & 1) + (q >> 1) * 4; st.o[m & 3] = mfma32(st.vfrag(m), asbf(pp[m >> 2]), st.o[m & 3]); }
    SBAR();
    STAMP(43);
    spin_all_gt(kvp, 31);
#pragma unroll
    for (int j = 0; j < 16; ++j) { const int mm = ((j >> 1) & 3) * 2 + (j >> 3); st.vread<3>(2 * mm + (j & 1)); }
    SBAR();
    auto fin = [&](int i, int s) {
      u32x4 w;
#pragma unroll
      for (int e = 0; e < 4; ++e) { const int r = 8 * s + 2 * e; w[e] = cvtpk(st.o[i][r] * f0 + st.o[2 + i][r] * f1, st.o[i][r + 1] * f0 + st.o[2 + i][r + 1] * f1); }
      of[0][2 * i + s] = w; st16(rs, o_out + ln_ * 16 + (2 * i + s) * 1024, w);
    };
#pragma unroll
    for (int q = 0; q < 8; ++q) {
      const int m = 2 * (q & 3) + (q >> 2);
      st.o[m & 3] = mfma32(st.vfrag(m), asbf((m & 2) ? pp[2 + (m >> 2)] : pq[6 + (m >> 2)]), st.o[m & 3]);
      if (q == 7) { st.release(); fin(0, 0); fin(0, 1); }
      SBAR();
    }
    fin(1, 0); fin(1, 1);
  }
  STAMP(44);
  f32x16 acc[2] = {f32x16{}, f32x16{}};
  {
    const int tadr = kh * 8192 + (4 * hh_ + qq_) * 128 + (((qq_ >> 1) ^ kh) << 6) + blk_ * 32 + p4_ * 8;
    const unsigned o_in = WS_O + (unsigned)((b * NH + oct * 8) * 24 + rb * 8 + 4 * kh) * 1024;
    int whave = 0;
    bf16x4 wf[2][16];
    auto op_step = [&](auto MMA, auto RD, auto LD, bf16x4 (&wc)[16], u32x4 (&oc)[4], int pn, bf16x4 (&wn)[16], unsigned src, u32x4 (&on)[4]) {
      if constexpr (RD.value) { if (whave <= pn) whave = spin_gt(lds + (pn < 3 ? C_WL + 4 * kh : C_WL2), pn); }
      LAS char* wb_ = lds + ((OSLOT >> (4 * pn)) & 15) * SLOT;
#pragma unroll
      for (int m = 0; m < 8; ++m) {
        if constexpr (MMA.value) acc[m & 1] = mfma32(cat8(wc[2 * m], wc[2 * m + 1]), asbf(oc[m >> 1]), acc[m & 1]);
        if constexpr (RD.value) {
          if (m < 6) {
#pragma unroll
            for (int i = 3 * m; i < 3 * m + 3 && i < 16; ++i) wn[i] = trread(wb_ + (tadr ^ (((i >> 1) & 1) << 6)) + (i >> 2) * 2048 + (i & 1) * 1024);
          }
        }
        if constexpr (LD.value) { if (m & 1) on[m >> 1] = ld16(rs, ln_ * 16 + (m >> 1) * 1024, src); }
        SBAR();
      }
    };
    lds_bump(post_addr(lds, C_OISS, ln_));
    VMW(0);
    STAMP(40);
    if (rfl(__hip_atomic_fetch_add((LAS int*)post_addr(lds, C_OCNT, ln_), 1, __ATOMIC_RELAXED, __HIP_MEMORY_SCOPE_WORKGROUP)) == 5)
      st16(rs, ln_ < 8 ? WS_F + (unsigned)wg * 128 + ln_ * 16 : WS_BYTES, u32x4{1u, 1u, 1u, 1u});
    SBAR();
    op_step(F{}, T{}, F{}, wf[1], of[0], 0, wf[0], 0u, of[0]);
    op_step(T{}, T{}, F{}, wf[0], of[0], 1, wf[1], 0u, of[0]);
    int hd[8]; unsigned src[8];
#pragma unroll
    for (int m = 1; m < 8; ++m) { hd[m] = unit_head(m, h8, b); src[m] = o_in + (unsigned)hd[m] * 24576; asm volatile("" : "+s"(hd[m]), "+s"(src[m])); }
    unsigned rdy = 0;
    auto need = [&](int m) { if (!((rdy >> hd[m]) & 1)) rdy = (unsigned)spin_bit(lds + C_OFL, hd[m]); CFENCE(); };
#pragma unroll
    for (int m = 1; m <= 2; ++m) {
      need(m);
#pragma unroll
      for (int ks = 0; ks < 4; ++ks) of[m][ks] = ld16(rs, ln_ * 16 + ks * 1024, src[m]);
      SBAR();
      if (m == 1) STAMP(46);
    }
#pragma unroll
    for (int c = 1; c < 8; ++c) {
      if (c < 6) { need(c + 2); op_step(T{}, T{}, T{}, wf[c & 1], of[c], c + 1, wf[(c + 1) & 1], src[(c + 2) & 7], of[(c + 2) & 7]); }
      else if (c < 7) op_step(T{}, T{}, F{}, wf[c & 1], of[c], c + 1, wf[(c + 1) & 1], 0u, of[0]);
      else op_step(T{}, F{}, F{}, wf[1], of[7], 0, wf[0], 0u, of[0]);
    }
  }
  STAMP(49);
  {
    LAS char* xo = lds + EXTRA + widx * 4096 + ln_ * 16;
    LAS char* po = lds + EXTRA + (widx ^ 1) * 4096 + ln_ * 16;
#pragma unroll
    for (int r4 = 0; r4 < 4; ++r4) *(LAS f32x4*)(xo + r4 * 1024) = f32x4{acc[1][4 * r4], acc[1][4 * r4 + 1], acc[1][4 * r4 + 2], acc[1][4 * r4 + 3]};
    LGKM0();
    lds_post(lds, C_RED + 4 * wave, 1, ln_);
    spin_gt(lds + C_RED + 4 * partner, 0);
    const unsigned p_out = WS_P + (unsigned)((b * NH + h8) * 24 + oct * 6 + widx) * 1024 + ln_ * 16;
    f32x4 t[4];
#pragma unroll
    for (int j = 0; j < 4; ++j) t[j] = *(LAS f32x4*)(po + j * 1024);
#pragma unroll
    for (int j = 0; j < 4; ++j) {
      const f32x4 v = {acc[0][4 * j] + t[j][0], acc[0][4 * j + 1] + t[j][1], acc[0][4 * j + 2] + t[j][2], acc[0][4 * j + 3] + t[j][3]};
      st16(rs, p_out + j * (8 * 24576), __builtin_bit_cast(u32x4, v));
    }
    STAMP(50);
    VMW(0);
    st16(rs, ln_ < 8 ? WS_G + (unsigned)(wg * 8 + widx) * 128 + ln_ * 16 : WS_BYTES, u32x4{1u, 1u, 1u, 1u});
    VMW(0);
#pragma unroll 1
    for (int spin = 0; spin < SPIN_MAX; ++spin) {
      CFENCE();
      if (__all(ld4(rs, (ln_ & 3) * 8192, WS_G + (unsigned)((b * NH + h8) * 8 + widx) * 128) == 1u)) break;
    }
    CFENCE();
    STAMP(41);
    u32x4 pv[4];
#pragma unroll
    for (int j = 0; j < 4; ++j) pv[j] = ld16(rs, ln_ * 16, p_in + j * 6144);
    const f32x4 s = (__builtin_bit_cast(f32x4, pv[0]) + __builtin_bit_cast(f32x4, pv[1])) + (__builtin_bit_cast(f32x4, pv[2]) + __builtin_bit_cast(f32x4, pv[3]));
    *(u32x2*)(out + ((size_t)b * SQ + 32 * rb + r32_) * DM + 64 * h8 + 32 * kh + 8 * oct + 4 * hh_) = u32x2{cvtpk(s[0], s[1]), cvtpk(s[2], s[3])};
  }
  STAMP(51);
}

extern "C" void kernel_launch(void* const* d_in, const int* in_sizes, int n_in, void* d_out, int out_size, void* d_ws, size_t ws_size,
                              hipStream_t stream) {
  (void)hipFuncSetAttribute((const void*)fused_kernel, hipFuncAttributeMaxDynamicSharedMemorySize, LDS_BYTES);
  hipLaunchKernelGGL(fused_kernel, dim3(256), dim3(512), LDS_BYTES, stream, (const __bf16*)d_in[0], (const __bf16*)d_in[1], (const __bf16*)d_in[2],
                     (const __bf16*)d_in[3], (const __bf16*)d_in[4], (__bf16*)d_out, (char*)d_ws);
}
```

```cpp
#include <hip/hip_runtime.h>
#include <hip/hip_bf16.h>
#include <stdint.h>
#include <type_traits>

#define LAS __attribute__((address_space(3)))
#define GAS __attribute__((address_space(1)))
#define DI __device__ __forceinline__
typedef __attribute__((ext_vector_type(4))) __bf16 bf16x4;
typedef __attribute__((ext_vector_type(8))) __bf16 bf16x8;
typedef __attribute__((ext_vector_type(2))) __bf16 bf16x2;
typedef __attribute__((ext_vector_type(4))) float f32x4;
typedef __attribute__((ext_vector_type(16))) float f32x16;
typedef __attribute__((ext_vector_type(2))) float f32x2;
typedef __attribute__((ext_vector_type(4))) unsigned u32x4;
typedef __attribute__((ext_vector_type(2))) unsigned u32x2;

constexpr int SQ = 96, DM = 512, NH = 32, SK = 1024, DH = 128;
constexpr int SLOT = 16384;
constexpr int KRING = 0, VRING = 4 * SLOT, EXTRA = 8 * SLOT;
constexpr int CTRL = EXTRA + 28672;
constexpr int C_WX = CTRL, C_KV = CTRL + 8, C_WL = CTRL + 16, C_WL2 = CTRL + 24, C_DONE = CTRL + 32, C_QRDY = CTRL + 64, C_MRG = CTRL + 96,
              C_RED = CTRL + 128, C_CLR = CTRL + 160, C_PEERS = CTRL + 192, C_OFL = CTRL + 200, C_OCNT = CTRL + 208, C_OISS = CTRL + 212,
              C_DUMMY = CTRL + 256;
constexpr int LDS_BYTES = 163840;
constexpr float SCL = 0.08838834764831845f * 1.4426950408889634f;
constexpr float PSUM_MAX = 4096.0f;
constexpr unsigned WBUF = 0x65840658u, XBUF = 0x73921739u;
constexpr unsigned OSLOT = 0x76210543u;
constexpr unsigned WS_O = 0, WS_P = 8u << 20, WS_H = 15u << 20, WS_F = WS_H + (64u << 10), WS_G = WS_F + (256u << 10), WS_BYTES = 16u << 20;
constexpr int SPIN_MAX = 1 << 16;
#define TAU 56

DI unsigned cvtpk(float lo, float hi) { f32x2 v = {lo, hi}; bf16x2 b = __builtin_convertvector(v, bf16x2); return __builtin_bit_cast(unsigned, b); }
DI f32x4 mfma16(bf16x8 a, bf16x8 b, f32x4 c) { return __builtin_amdgcn_mfma_f32_16x16x32_bf16(a, b, c, 0, 0, 0); }
DI f32x16 mfma32(bf16x8 a, bf16x8 b, f32x16 c) { return __builtin_amdgcn_mfma_f32_32x32x16_bf16(a, b, c, 0, 0, 0); }
DI bf16x4 trread(LAS char* p) { return __builtin_amdgcn_ds_read_tr16_b64_v4bf16((LAS bf16x4*)p); }
DI bf16x8 cat8(bf16x4 a, bf16x4 b) { return __builtin_shufflevector(a, b, 0, 1, 2, 3, 4, 5, 6, 7); }
DI bf16x8 asbf(const u32x4& w) { return __builtin_bit_cast(bf16x8, w); }
DI float xmax2(float v) {
  auto s = __builtin_amdgcn_permlane32_swap(__float_as_uint(v), __float_as_uint(v), false, false);
  return fmaxf(__uint_as_float(s[0]), __uint_as_float(s[1]));
}
DI u32x4 exp8(const f32x16& s, int o) {
  u32x4 w;
#pragma unroll
  for (int i = 0; i < 4; ++i) w[i] = cvtpk(__builtin_amdgcn_exp2f(s[o + 2 * i]), __builtin_amdgcn_exp2f(s[o + 2 * i + 1]));
  return w;
}
#define CFENCE() asm volatile("" ::: "memory")
#define LGKM0() do { CFENCE(); __builtin_amdgcn_s_waitcnt(0xc07f); CFENCE(); } while (0)
#define VMW(N) asm volatile("s_waitcnt vmcnt(" #N ")" ::: "memory")
#define SBAR() __builtin_amdgcn_sched_barrier(0)
#define PIN(x) asm volatile("" : "+v"(x))
DI int rfl(int v) { return __builtin_amdgcn_readfirstlane(v); }
DI int lds_word(LAS char* p) { return rfl(*(volatile LAS int*)p); }
DI int lds_min2(LAS char* p) { u32x2 v = *(volatile LAS u32x2*)p; return rfl(min((int)v[0], (int)v[1])); }
DI int lds_min6(LAS char* p) {
  u32x4 v = *(volatile LAS u32x4*)p; u32x2 w = *(volatile LAS u32x2*)(p + 16);
  return rfl(min(min(min((int)v[0], (int)v[1]), min((int)v[2], (int)v[3])), min((int)w[0], (int)w[1])));
}
DI LAS char* post_addr(LAS char* lds, int off, int lane) { return lds + (lane == 0 ? off : C_DUMMY + 4 * lane); }
DI void lds_post(LAS char* lds, int off, int v, int lane) { *(volatile LAS int*)post_addr(lds, off, lane) = v; }
DI void lds_bump(LAS char* p) { __hip_atomic_fetch_add((LAS int*)p, 1, __ATOMIC_RELAXED, __HIP_MEMORY_SCOPE_WORKGROUP); }
typedef __amdgpu_buffer_rsrc_t rsrc_t;
DI void st16(rsrc_t r, unsigned off, u32x4 v) { __builtin_amdgcn_raw_buffer_store_b128(v, r, (int)off, 0, 16); }
DI u32x4 ld16(rsrc_t r, unsigned voff, unsigned soff) { return __builtin_amdgcn_raw_buffer_load_b128(r, (int)voff, (int)soff, 16); }
DI void st4(rsrc_t r, unsigned off, unsigned v) { __builtin_amdgcn_raw_buffer_store_b32(v, r, (int)off, 0, 16); }
DI unsigned ld4(rsrc_t r, unsigned voff, unsigned soff) { return __builtin_amdgcn_raw_buffer_load_b32(r, (int)voff, (int)soff, 16); }
DI int fresh_lane() { int l; asm volatile("v_mbcnt_lo_u32_b32 %0, -1, 0\n\tv_mbcnt_hi_u32_b32 %0, -1, %0" : "=v"(l)); return l; }
DI int unit_head(int pos, int h8, int b) { int n = pos - 1 + 2 * (b & 3); n = n >= 7 ? n - 7 : n; return pos == 0 ? h8 : ((h8 + 1 + n) & 7); }

#define STAMP(i) do {} while (0)

DI int spin_gt(LAS char* p, int n) {
  int v, r;
  asm volatile("1:\n\tds_read_b32 %1, %2\n\ts_waitcnt lgkmcnt(0)\n\tv_readfirstlane_b32 %0, %1\n\ts_cmp_le_i32 %0, %3\n\ts_cbranch_scc1 1b"
               : "=&s"(r), "=&v"(v) : "v"(p), "s"(n) : "memory", "scc");
  return r;
}
DI void spin_all_gt(LAS char* p, int n) {
  int v;
  asm volatile("1:\n\tds_read_b32 %0, %1\n\ts_waitcnt lgkmcnt(0)\n\tv_cmp_ge_i32 vcc, %2, %0\n\ts_cbranch_vccnz 1b"
               : "=&v"(v) : "v"(p), "v"(n) : "memory", "vcc");
}
DI int spin_bit(LAS char* p, int bit) {
  int v, r;
  asm volatile("1:\n\tds_read_b32 %1, %2\n\ts_waitcnt lgkmcnt(0)\n\tv_readfirstlane_b32 %0, %1\n\ts_bitcmp0_b32 %0, %3\n\ts_cbranch_scc1 1b"
               : "=&s"(r), "=&v"(v) : "v"(p), "s"(bit) : "memory", "scc");
  return r;
}
DI void wait_gt(int& have, int n, LAS char* p) {
  while (have <= n) { have = lds_word(p); if (have <= n) __builtin_amdgcn_s_sleep(1); }
  CFENCE();
}

struct Attn {
  f32x16 o[4];
  f32x16 negm;
  bf16x8 qf[8], kf[8];
  bf16x4 vf[16];
  u32x4 pf[2];
  bf16x8 ones;
  float lsum;
  int have;
  LAS char *kb[8], *vb[4], *land, *donep;

  DI void release() { LGKM0(); lds_bump(donep); }
  template <int S> DI void kread(int t) { kf[t] = *(LAS bf16x8*)(kb[t] + S * SLOT); }
  template <int S> DI void vread(int i) { vf[i] = trread(vb[(i >> 1) & 3] + S * SLOT + (i >> 3) * 4096 + (i & 1) * 2048); }
  DI bf16x8 vfrag(int m) { return cat8(vf[2 * m], vf[2 * m + 1]); }
  DI float rowmax(const f32x16& s) {
    float mx = s[0];
#pragma unroll
    for (int r = 1; r < 16; ++r) mx = fmaxf(mx, s[r]);
    return xmax2(mx);
  }
  DI f32x4 psum(const u32x4& p0, const u32x4& p1) { return mfma16(ones, asbf(p1), mfma16(ones, asbf(p0), f32x4{0.f, 0.f, 0.f, 0.f})); }
  template <int S> DI void tile(f32x16& cur, f32x16& nxt, int i) {
    release(); wait_gt(have, 2 * i + 2, land);
    SBAR();
    f32x4 ts = {0.f, 0.f, 0.f, 0.f};
    float ea, eb, pa = 0.f, pb = 0.f;
#pragma unroll
    for (int m = 0; m < 8; ++m) {
      nxt = mfma32(kf[m], qf[m], m == 0 ? negm : nxt);
      if (m < 5) { vread<S>(3 * m); vread<S>(3 * m + 1); vread<S>(3 * m + 2); }
      if (m == 5) vread<S>(15);
      ea = __builtin_amdgcn_exp2f(cur[2 * m]); eb = __builtin_amdgcn_exp2f(cur[2 * m + 1]);
      asm volatile("" : "+v"(ea), "+v"(eb));
      if (m > 0) { unsigned w = cvtpk(pa, pb); PIN(w); pf[(m - 1) >> 2][(m - 1) & 3] = w; }
      pa = ea; pb = eb;
      if (m == 6) ts = mfma16(ones, asbf(pf[0]), ts);
      SBAR();
    }
    pf[1][3] = cvtpk(pa, pb);
    ts = mfma16(ones, asbf(pf[1]), ts);
    release(); wait_gt(have, 2 * i + 3, land);
    if (__builtin_expect(__any(!(ts[0] <= PSUM_MAX)), 0)) {
      const float dl = fmaxf(rowmax(cur), 0.f);
      const float al = __builtin_amdgcn_exp2f(-dl);
#pragma unroll
      for (int r = 0; r < 16; ++r) { cur[r] -= dl; nxt[r] -= dl; negm[r] -= dl; }
      PIN(negm);
      pf[0] = exp8(cur, 0); pf[1] = exp8(cur, 8);
      ts = psum(pf[0], pf[1]);
      lsum *= al;
#pragma unroll
      for (int c = 0; c < 4; ++c) o[c] *= al;
      PIN(lsum); PIN(ts); PIN(nxt); PIN(pf[0]); PIN(pf[1]);
    }
    PIN(lsum); PIN(ts);
    lsum += ts[0];
    SBAR();
#pragma unroll
    for (int m = 0; m < 8; ++m) {
      o[m & 3] = mfma32(vfrag(m), asbf(pf[m >> 2]), o[m & 3]);
      if (m < 4) { kread<(S + 2) & 3>(2 * m); kread<(S + 2) & 3>(2 * m + 1); }
      SBAR();
    }
  }
};

__global__ __launch_bounds__(512, 2) void fused_kernel(const __bf16* __restrict__ x, const __bf16* __restrict__ kc, const __bf16* __restrict__ vc,
                                                       const __bf16* __restrict__ wq, const __bf16* __restrict__ wo, __bf16* __restrict__ out, char* ws) {
  extern __shared__ __attribute__((aligned(16))) char smem[];
  LAS char* lds = (LAS char*)smem;
  const int tid = threadIdx.x, lane = tid & 63, wave = (0x54321076u >> (4 * rfl(tid >> 6))) & 15;
  const int bid = blockIdx.x, xcd = bid & 7, idx = bid >> 3;
  const int oct = xcd & 3, h8 = idx & 7;
  const int h = oct * 8 + h8, b = (xcd >> 2) * 4 + (idx >> 3);
  const int wg = b * NH + h;
  using T = std::true_type; using F = std::false_type;
  STAMP(0);

  if (__builtin_expect(wave >= 6, 1)) {
    __builtin_amdgcn_s_setprio(3);
    const int L = wave - 6;
    const int r16 = lane & 15, g = lane >> 4;
    const char* wp = (const char*)(wq + h * DH) + (size_t)L * 32 * 8192;
    const char* xp = (const char*)(x + ((size_t)b * SQ + 48 * L) * DM);
    unsigned oa[4], ob[4];
#pragma unroll
    for (int i = 0; i < 4; ++i) {
      oa[i] = i * (32768 - 1024) + g * 8192 + ((r16 ^ (g << 2)) << 4);
      const int row = 8 * i + (lane >> 3);
      ob[i] = row * 1024 - i * 1024 + (((lane & 7) ^ ((row >> 1) & 7)) << 4);
    }
    LAS char* lb = lds + L * 8192;
    int seen = 0;
#define GLDS(src, dst, off, aux) __builtin_amdgcn_global_load_lds((const GAS void*)(src), (LAS void*)(dst), 16, off, aux)
#define GLDS4(src, o0, o1, o2, o3, dst, aux) do { GLDS((src) + (o0), dst, 0, aux); GLDS((src) + (o1), dst, 1024, aux); GLDS((src) + (o2), dst, 2048, aux); GLDS((src) + (o3), dst, 3072, aux); } while (0)
#define POST(p, v) (*(volatile LAS int*)(p) = (v))
    auto wait_done = [&](int need) {
      while (seen < need) { seen = lds_min6(lds + C_DONE); if (seen < need) __builtin_amdgcn_s_sleep(1); }
      CFENCE();
    };
    LAS char* pw = post_addr(lds, C_WX + 4 * L, lane);
#pragma unroll 1
    for (int cc = 0; cc < 8; ++cc) {
      wait_done(cc - 4);
      LAS char* xd = lds + ((XBUF >> (4 * cc)) & 15) * SLOT + L * 6144;
      LAS char* wd = lb + ((WBUF >> (4 * cc)) & 15) * SLOT;
      const char* xs = xp + cc * 128;
      GLDS4(xs, ob[0], ob[1], ob[2], ob[3], xd, 0);
      GLDS(xs + 32768 + ob[0], xd + 4096, 0, 0); GLDS(xs + 32768 + ob[1], xd + 4096, 1024, 0);
      if (cc >= 2) { VMW(20); POST(pw, cc - 1); }
      const char* wsrc = wp + (size_t)cc * 64 * 8192;
      GLDS4(wsrc, oa[0], oa[1], oa[2], oa[3], wd, 0);
      GLDS4(wsrc + 16 * 8192, oa[0], oa[1], oa[2], oa[3], wd + 4096, 0);
      STAMP(1 + cc);
      if (cc == 0) asm volatile("s_barrier" ::: "memory");
    }
    const char* kp = (const char*)(kc + (size_t)wg * SK * DH) + L * 8192;
    const char* vp = (const char*)(vc + (size_t)wg * SK * DH) + L * 8192;
    const unsigned ov = g * 256 + ((r16 ^ (g << 2)) << 4);
#pragma unroll
    for (int i = 0; i < 4; ++i) {
      oa[i] = g * 256 + ((r16 ^ (4 * i + g)) << 4);
      const int row = 8 * i + (lane >> 3);
      ob[i] = row * 1024 - i * 1024 + (((lane & 7) ^ (((row >> 1) & 1) << 2)) << 4);
    }
    auto issue_k = [&](int n) {
      const char* src = kp + n * SLOT; LAS char* dst = lb + KRING + (n & 3) * SLOT;
      GLDS4(src, oa[0], oa[1], oa[2], oa[3], dst, 2); GLDS4(src + 4096, oa[0], oa[1], oa[2], oa[3], dst + 4096, 2);
    };
    auto issue_v = [&](int n) {
      const char* src = vp + n * SLOT; LAS char* dst = lb + VRING + (n & 3) * SLOT;
      GLDS4(src, ov, ov, ov, ov, dst, 2); GLDS4(src + 4096, ov, ov, ov, ov, dst + 4096, 2);
    };
    wait_done(4);
    unsigned due = (unsigned)__builtin_amdgcn_s_memrealtime() + 2 * TAU;
    issue_k(0);
    STAMP(17);
    VMW(22); POST(pw, 7);
    issue_k(1);
    VMW(16); POST(pw, 8);
    LAS char* pk = post_addr(lds, C_KV + 4 * L, lane);
    auto gate = [&](int j, int need) {
      if (seen < need) {
        seen = lds_min6(lds + C_DONE);
        if (seen < need) {
          VMW(0); POST(pk, j);
          do { __builtin_amdgcn_s_sleep(1); seen = lds_min6(lds + C_DONE); } while (seen < need);
        }
      }
      while ((int)((unsigned)__builtin_amdgcn_s_memrealtime() - due) < 0) __builtin_amdgcn_s_sleep(1);
      due += TAU;
      CFENCE();
    };
#define PUBLISH(j) do { VMW(8); POST(pk, j); } while (0)
#pragma unroll 1
    for (int n = 0; n < 12; ++n) {
      const int j = 2 * n + 2;
      gate(j, n < 4 ? ((0x9875 >> (4 * n)) & 15) : j + 1);
      issue_v(n); PUBLISH(j);
      gate(j + 1, n < 3 ? ((0x975 >> (4 * n)) & 15) : j + 2);
      issue_k(n + 2); PUBLISH(j + 1);
      STAMP(18 + n);
    }
    gate(26, 28); issue_k(14); PUBLISH(26);
    gate(27, 30); issue_k(15); PUBLISH(27);
    gate(28, 30); issue_v(12); PUBLISH(28);
    gate(29, 30); issue_v(13); PUBLISH(29);
    gate(30, 31); issue_v(14); PUBLISH(30);
    gate(31, 33); issue_v(15);
    STAMP(48);
    PUBLISH(31);
    const char* op = (const char*)(wo + (size_t)(oct * 1024 + 64 * L) * DM + h8 * 64);
    auto issue_o = [&](int pos) {
      const char* src = op + (size_t)unit_head(pos, h8, b) * 128 * 1024; LAS char* dst = lb + ((OSLOT >> (4 * pos)) & 15) * SLOT;
      GLDS4(src, ob[0], ob[1], ob[2], ob[3], dst, 0); GLDS4(src + 32768, ob[0], ob[1], ob[2], ob[3], dst + 4096, 0);
    };
    LAS char* pl = post_addr(lds, C_WL + 4 * L, lane);
    __builtin_amdgcn_s_setprio(0);
    wait_done(36);
    issue_o(0);
    PUBLISH(32);
    STAMP(49);
    wait_done(37); issue_o(1);
    while (lds_word(lds + C_OISS) < 6) __builtin_amdgcn_s_sleep(1);
    wait_done(41);
    issue_o(2);
    VMW(8); POST(pl, 2);
    if (L == 1) {
      LAS char* pl2 = post_addr(lds, C_WL2, lane);
#pragma unroll 1
      for (int pos = 3; pos < 8; ++pos) {
        issue_o(pos);
        op -= 65536; lb -= 8192; issue_o(pos); op += 65536; lb += 8192;
        VMW(16); POST(pos == 3 ? pl : pl2, pos);
      }
      VMW(0); POST(pl2, 8);
    } else {
      VMW(0); POST(pl, 3);
      const rsrc_t rs = __builtin_amdgcn_make_buffer_rsrc(ws, 0, WS_BYTES, 0x00020000);
      if (lds_word(lds + C_PEERS) == 0) {
        const bool peer = lane < 32 && ((lane >> 3) == oct || (lane & 7) == h8);
        const unsigned hello_to = peer ? WS_H + (unsigned)(b * NH + lane) * 128 + h * 4 : WS_BYTES;
        for (int spin = 0; spin < SPIN_MAX; ++spin) {
          st4(rs, hello_to, 1u); __builtin_amdgcn_s_sleep(8);
          if (!__any(peer && ld4(rs, (lane & 31) * 4, WS_H + (unsigned)wg * 128) != 1u)) break;
        }
        st4(rs, hello_to, 1u);
      }
      while (lds_word(lds + C_OCNT) < 6) __builtin_amdgcn_s_sleep(2);
      LAS char* pm = post_addr(lds, C_OFL, lane);
      for (int spin = 0; spin < SPIN_MAX; ++spin) {
        CFENCE();
        const unsigned m = (unsigned)__builtin_amdgcn_ballot_w64(ld4(rs, (lane & 7) * 128, WS_F + (unsigned)(b * NH + oct * 8) * 128) == 1u) & 0xffu;
        POST(pm, spin == SPIN_MAX - 1 ? 0xff : (int)m);
        if (m == 0xffu) break;
        __builtin_amdgcn_s_sleep(2);
      }
    }
    STAMP(58);
    return;
  }
  const int kh = wave >= 3, rb = wave < 3 ? wave : (wave == 3 ? 2 : wave - 4);
  const int partner = rb == 2 ? (wave ^ 1) : (wave ^ 4);
  const int widx = 2 * rb + kh;
  if (wave == 0) *(LAS int*)(lds + CTRL + 4 * lane) = 0;
  asm volatile("s_waitcnt lgkmcnt(0)\n\ts_barrier" ::: "memory");
  const int r32 = lane & 31, hh = lane >> 5, blk = (lane >> 4) & 1, qq = (lane & 15) >> 2, p4 = lane & 3;
  const rsrc_t rs = __builtin_amdgcn_make_buffer_rsrc(ws, 0, WS_BYTES, 0x00020000);
  const unsigned o_out = WS_O + (unsigned)((wg * 3 + rb) * 8 + 4 * kh) * 1024;
  Attn st;
  st.donep = post_addr(lds, C_DONE + 4 * wave, lane);
  {
    f32x16 qa[2] = {f32x16{}, f32x16{}};
    const int wadr = (8 * hh + qq) * 256 + (((4 * kh + blk) ^ (qq << 1)) << 5) + p4 * 8;
    const int xrow = 32 * rb + r32;
    const int xadr = xrow * 128 + ((hh ^ ((xrow >> 1) & 7)) << 4);
    bf16x8 xa[4], xb[4];
    bf16x4 wa[16], wb[16];
    LAS char* wxp = lds + C_WX + 4 * (lane & 1);
    auto qp_step = [&](auto MMA, auto RD, bf16x8 (&xc)[4], bf16x4 (&wc)[16], int cn, bf16x8 (&xn)[4], bf16x4 (&wn)[16]) {
      LAS char* xb_ = lds + ((XBUF >> (4 * cn)) & 15) * SLOT;
      LAS char* wb_ = lds + ((WBUF >> (4 * cn)) & 15) * SLOT;
      int fl = 0;
#pragma unroll
      for (int m = 0; m < 8; ++m) {
        if constexpr (MMA.value) qa[m & 1] = mfma32(cat8(wc[2 * m], wc[2 * m + 1]), xc[m >> 1], qa[m & 1]);
        if constexpr (RD.value) {
          constexpr int lo[7] = {0, 4, 8, 11, 14, 17, 20};
          if (m < 6) {
#pragma unroll
            for (int i = lo[m]; i < lo[m + 1]; ++i) {
              const int ks = i / 5, e = i % 5 - 1;
              if (e < 0) xn[ks] = *(LAS bf16x8*)(xb_ + (xadr ^ (ks << 5)));
              else wn[4 * ks + e] = trread(wb_ + (wadr ^ ((e >> 1) << 6)) + ks * 4096 + (e & 1) * 1024);
            }
          }
          if (m == 5) fl = *(volatile LAS int*)wxp;
        }
        SBAR();
      }
      if constexpr (RD.value) {
        st.release();
        if (cn < 7 && __any(fl <= cn + 1)) spin_all_gt(wxp, cn + 1);
        CFENCE();
      }
    };
    while (lds_min2(lds + C_WX) < 1) __builtin_amdgcn_s_sleep(1);
    CFENCE();
    STAMP(1);
    qp_step(F{}, T{}, xb, wb, 0, xa, wa);
#pragma unroll 1
    for (int cc = 0; cc < 6; cc += 2) {
      qp_step(T{}, T{}, xa, wa, cc + 1, xb, wb);
      qp_step(T{}, T{}, xb, wb, cc + 2, xa, wa);
    }
    STAMP(8);
    qp_step(T{}, T{}, xa, wa, 7, xb, wb);
    qp_step(T{}, F{}, xb, wb, 0, xa, wa);
    spin_all_gt(lds + C_DONE + 4 * min(lane, 5), 5);
    LAS char* ql = lds + EXTRA + xrow * 256;
#pragma unroll
    for (int i = 0; i < 2; ++i)
#pragma unroll
      for (int r4 = 0; r4 < 4; ++r4) {
        const int ch = 4 * (2 * kh + i) + r4;
        u32x2 w = {cvtpk(qa[i][4 * r4] * SCL, qa[i][4 * r4 + 1] * SCL), cvtpk(qa[i][4 * r4 + 2] * SCL, qa[i][4 * r4 + 3] * SCL)};
        *(LAS u32x2*)(ql + ((ch ^ (r32 & 15)) << 4) + hh * 8) = w;
      }
    LGKM0();
    lds_post(lds, C_QRDY + 4 * wave, 1, lane);
    spin_all_gt(lds + (lane ? C_KV + 4 * kh : C_QRDY + 4 * partner), lane ? 1 : 0);
#pragma unroll
    for (int t = 0; t < 8; ++t) st.qf[t] = *(LAS bf16x8*)(ql + (((2 * t + hh) ^ (r32 & 15)) << 4));
  }
  STAMP(9);
  {
    const int kadr = KRING + kh * 8192 + r32 * 256 + ((hh ^ (r32 & 15)) << 4);
    const int vadr = VRING + kh * 8192 + (4 * hh + qq) * 256 + ((blk ^ (qq << 1)) << 5) + p4 * 8;
#pragma unroll
    for (int t = 0; t < 8; ++t) st.kb[t] = lds + (kadr ^ (t << 5));
#pragma unroll
    for (int c = 0; c < 4; ++c) st.vb[c] = lds + (vadr ^ ((c ^ (2 * kh)) << 6));
    const unsigned one = (blk == (qq & 1)) ? 0x3f803f80u : 0u;
    st.ones = asbf(u32x4{one, one, one, one});
  }
  st.land = lds + C_KV + 4 * kh;
#pragma unroll
  for (int c = 0; c < 4; ++c) st.o[c] = f32x16{};
  st.lsum = 0.f; st.have = 0;
  f32x16 sA, sB;
  {
#pragma unroll
    for (int t = 0; t < 8; ++t) st.kread<0>(t);
    SBAR();
    sA = f32x16{};
#pragma unroll
    for (int t = 0; t < 8; ++t) sA = mfma32(st.kf[t], st.qf[t], sA);
    SBAR();
    st.release();
#pragma unroll
    for (int t = 0; t < 8; ++t) st.kread<1>(t);
    SBAR();
    const float mx = st.rowmax(sA);
#pragma unroll
    for (int r = 0; r < 16; ++r) { sA[r] -= mx; st.negm[r] = -mx; }
  }
  auto hello = [&]() {
    const int ln = fresh_lane();
    const bool peer = ln < 32 && ((ln >> 3) == oct || (ln & 7) == h8);
    st4(rs, peer ? WS_H + (unsigned)(b * NH + ln) * 128 + h * 4 : WS_BYTES, 1u);
  };
  STAMP(10);
#pragma unroll 1
  for (int it = 0; it < 12; it += 4) {
    st.tile<0>(sA, sB, it); st.tile<1>(sB, sA, it + 1); st.tile<2>(sA, sB, it + 2); st.tile<3>(sB, sA, it + 3);
    if (wave == 2) {
      if (it == 0) {
        const int ln = fresh_lane();
        st16(rs, ln < 48 ? WS_G + (unsigned)wg * 1024 + ln * 16 : (ln < 56 ? WS_F : WS_H) + (unsigned)wg * 128 + (ln & 7) * 16, u32x4{0u, 0u, 0u, 0u});
      } else if (it == 4) {
        VMW(0);
        hello();
      }
    }
    STAMP(11 + (it >> 2));
  }
  const int ln_ = fresh_lane();
  const int r32_ = ln_ & 31, hh_ = ln_ >> 5, blk_ = (ln_ >> 4) & 1, qq_ = (ln_ & 15) >> 2, p4_ = ln_ & 3;
  u32x4 of[8][4];
  {
    unsigned hin = 0;
    if (wave == 2) hin = ld4(rs, (ln_ & 31) * 4, WS_H + (unsigned)wg * 128);
    u32x4 pq[8];
    auto sm_step = [&](auto GG, f32x16& cur, f32x16& nxt) {
      constexpr int G = GG.value;
      if constexpr (G < 3) st.release();
      SBAR();
#pragma unroll
      for (int t = 0; t < 8; ++t) {
        if constexpr (G < 3) nxt = mfma32(st.kf[t], st.qf[t], t == 0 ? st.negm : nxt);
        unsigned w = cvtpk(__builtin_amdgcn_exp2f(cur[2 * t]), __builtin_amdgcn_exp2f(cur[2 * t + 1]));
        PIN(w); pq[2 * G + (t >> 2)][t & 3] = w;
        SBAR();
      }
      f32x4 ts = st.psum(pq[2 * G], pq[2 * G + 1]);
      if constexpr (G < 2) {
        wait_gt(st.have, 26 + G, st.land);
#pragma unroll
        for (int t = 0; t < 8; ++t) st.kread<2 + G>(t);
      }
      SBAR();
      if (__builtin_expect(__any(!(ts[0] <= PSUM_MAX)), 0)) {
        const float dl = __builtin_ceilf(fmaxf(st.rowmax(cur), 0.f));
        const float al = __builtin_amdgcn_exp2f(-dl);
#pragma unroll
        for (int r = 0; r < 16; ++r) { cur[r] -= dl; if (G < 3) nxt[r] -= dl; st.negm[r] -= dl; }
        PIN(st.negm);
        pq[2 * G] = exp8(cur, 0); pq[2 * G + 1] = exp8(cur, 8);
        ts = st.psum(pq[2 * G], pq[2 * G + 1]);
        st.lsum *= al;
#pragma unroll
        for (int c = 0; c < 4; ++c) st.o[c] *= al;
#pragma unroll
        for (int i = 0; i < 8 * G; ++i) { const unsigned w = pq[i >> 2][i & 3]; pq[i >> 2][i & 3] = cvtpk(__uint_as_float(w << 16) * al, __uint_as_float(w & 0xffff0000u) * al); }
        PIN(st.lsum); PIN(ts); if (G < 3) PIN(nxt);
#pragma unroll
        for (int i = 0; i < 2 * G + 2; ++i) PIN(pq[i]);
      }
      PIN(st.lsum); PIN(ts);
      st.lsum += ts[0];
    };
    sm_step(std::integral_constant<int, 0>{}, sA, sB);
    sm_step(std::integral_constant<int, 1>{}, sB, sA);
    if (wave == 2) {
      if (!__any(ln_ < 32 && ((ln_ >> 3) == oct || (ln_ & 7) == h8) && hin != 1u)) { hello(); lds_post(lds, C_PEERS, 1, ln_); }
    }
    sm_step(std::integral_constant<int, 2>{}, sA, sB);
    sm_step(std::integral_constant<int, 3>{}, sB, sA);
    const float lsum = st.lsum, mrun = -st.negm[0];
    {
      LAS char* xp_ = lds + EXTRA + widx * 4096 + ln_ * 16;
#pragma unroll
      for (int i = 0; i < 4; ++i) *(LAS u32x4*)(xp_ + i * 1024) = pq[4 + i];
      *(LAS f32x2*)(lds + EXTRA + 24576 + widx * 512 + ln_ * 8) = f32x2{mrun, lsum};
    }
    LGKM0();
    lds_post(lds, C_MRG + 4 * wave, 1, ln_);
    STAMP(39);
    wait_gt(st.have, 28, st.land);
#pragma unroll
    for (int j = 0; j < 16; ++j) st.vread<0>(j);
    SBAR();
#pragma unroll
    for (int m = 0; m < 8; ++m) st.o[m & 3] = mfma32(st.vfrag(m), asbf(pq[m >> 2]), st.o[m & 3]);
    st.release();
    SBAR();
    LAS char* mrgp = lds + C_MRG + 4 * partner;
    spin_all_gt(ln_ < 6 ? lds + C_DONE + 4 * ln_ : mrgp, ln_ < 6 ? 34 : 0);
    u32x4 pp[4];
    const f32x2 ml = *(LAS f32x2*)(lds + EXTRA + 24576 + (widx ^ 1) * 512 + ln_ * 8);
    {
      LAS char* pp_ = lds + EXTRA + (widx ^ 1) * 4096 + ln_ * 16;
#pragma unroll
      for (int i = 0; i < 4; ++i) pp[i] = *(LAS u32x4*)(pp_ + i * 1024);
    }
    if (st.have <= 29) spin_gt(st.land, 29);
#pragma unroll
    for (int j = 0; j < 16; ++j) st.vread<1>(j ^ 4);
    const float mn = fmaxf(mrun, ml[0]);
    const float a0 = __builtin_amdgcn_exp2f(mrun - mn), a1 = __builtin_amdgcn_exp2f(ml[0] - mn);
    const float inv = 1.0f / (a0 * lsum + a1 * ml[1]);
    float f0 = a0 * inv, f1 = a1 * inv;
    PIN(f0); PIN(f1);
    SBAR();
    LAS char* xo = lds + KRING + widx * 8192 + ln_ * 16;
#pragma unroll
    for (int m = 0; m < 8; ++m) {
      const int mm = m < 2 ? m + 2 : (m < 4 ? m + 4 : (m < 6 ? m - 4 : m - 2));
      st.o[mm & 3] = mfma32(st.vfrag(mm), asbf(pq[2 + (mm >> 2)]), st.o[mm & 3]);
      if (m >= 5) {
#pragma unroll
        for (int q = (m - 5) * 3; q < (m - 5) * 3 + 3 && q < 8; ++q) {
          const f32x16& v = st.o[2 + (q >> 2)]; const int r = 4 * (q & 3);
          *(LAS f32x4*)(xo + q * 1024) = f32x4{v[r], v[r + 1], v[r + 2], v[r + 3]};
        }
      }
      SBAR();
    }
    st.release();
    lds_post(lds, C_MRG + 4 * wave, 2, ln_);
    STAMP(42);
    st.vb[2] = st.vb[0] + (kh ? -8192 : 8192); st.vb[3] = st.vb[1] + (kh ? -8192 : 8192);
    LAS char* kvp = lds + C_KV + 4 * (ln_ & 1);
    LAS char* po = lds + KRING + (widx ^ 1) * 8192 + ln_ * 16;
    auto get_o = [&]() {
#pragma unroll
      for (int q = 0; q < 8; ++q) {
        const f32x4 t = *(volatile LAS f32x4*)(po + q * 1024);
#pragma unroll
        for (int r = 0; r < 4; ++r) st.o[2 + (q >> 2)][4 * (q & 3) + r] = t[r];
      }
    };
    spin_all_gt(kvp, 30);
#pragma unroll
    for (int j = 0; j < 16; ++j) st.vread<2>((j & 3) + ((j >> 2) & 1) * 8 + (j >> 3) * 4);
    const int fl = *(volatile LAS int*)mrgp;
    get_o();
    SBAR();
#pragma unroll
    for (int q = 0; q < 4; ++q) { const int m = (q & 1) + (q >> 1) * 4; st.o[m & 3] = mfma32(st.vfrag(m), asbf(pq[4 + (m >> 2)]), st.o[m & 3]); }
    SBAR();
    if (rfl(fl) < 2) { spin_gt(mrgp, 1); get_o(); }
    st.release(); lds_bump(st.donep);
#pragma unroll
    for (int q = 0; q < 4; ++q) { const int m = 2 + (q & 1) + (q >> 1) * 4; st.o[m & 3] = mfma32(st.vfrag(m), asbf(pp[m >> 2]), st.o[m & 3]); }
    SBAR();
    STAMP(43);
    spin_all_gt(kvp, 31);
#pragma unroll
    for (int j = 0; j < 16; ++j) { const int mm = ((j >> 1) & 3) * 2 + (j >> 3); st.vread<3>(2 * mm + (j & 1)); }
    SBAR();
    auto fin = [&](int i, int s) {
      u32x4 w;
#pragma unroll
      for (int e = 0; e < 4; ++e) { const int r = 8 * s + 2 * e; w[e] = cvtpk(st.o[i][r] * f0 + st.o[2 + i][r] * f1, st.o[i][r + 1] * f0 + st.o[2 + i][r + 1] * f1); }
      of[0][2 * i + s] = w; st16(rs, o_out + ln_ * 16 + (2 * i + s) * 1024, w);
    };
#pragma unroll
    for (int q = 0; q < 8; ++q) {
      const int m = 2 * (q & 3) + (q >> 2);
      st.o[m & 3] = mfma32(st.vfrag(m), asbf((m & 2) ? pp[2 + (m >> 2)] : pq[6 + (m >> 2)]), st.o[m & 3]);
      if (q == 7) { st.release(); fin(0, 0); fin(0, 1); }
      SBAR();
    }
    fin(1, 0); fin(1, 1);
  }
  STAMP(44);
  f32x16 acc[2] = {f32x16{}, f32x16{}};
  {
    const int tadr = kh * 8192 + (4 * hh_ + qq_) * 128 + (((qq_ >> 1) ^ kh) << 6) + blk_ * 32 + p4_ * 8;
    const unsigned o_in = WS_O + (unsigned)((b * NH + oct * 8) * 24 + rb * 8 + 4 * kh) * 1024;
    int whave = 0;
    bf16x4 wf[2][16];
    auto op_step = [&](auto MMA, auto RD, auto LD, bf16x4 (&wc)[16], u32x4 (&oc)[4], int pn, bf16x4 (&wn)[16], unsigned src, u32x4 (&on)[4]) {
      if constexpr (RD.value) { if (whave <= pn) whave = spin_gt(lds + (pn < 3 ? C_WL + 4 * kh : C_WL2), pn); }
      LAS char* wb_ = lds + ((OSLOT >> (4 * pn)) & 15) * SLOT;
#pragma unroll
      for (int m = 0; m < 8; ++m) {
        if constexpr (MMA.value) acc[m & 1] = mfma32(cat8(wc[2 * m], wc[2 * m + 1]), asbf(oc[m >> 1]), acc[m & 1]);
        if constexpr (RD.value) {
          if (m < 6) {
#pragma unroll
            for (int i = 3 * m; i < 3 * m + 3 && i < 16; ++i) wn[i] = trread(wb_ + (tadr ^ (((i >> 1) & 1) << 6)) + (i >> 2) * 2048 + (i & 1) * 1024);
          }
        }
        if constexpr (LD.value) { if (m & 1) on[m >> 1] = ld16(rs, ln_ * 16 + (m >> 1) * 1024, src); }
        SBAR();
      }
    };
    lds_bump(post_addr(lds, C_OISS, ln_));
    VMW(0);
    STAMP(40);
    if (rfl(__hip_atomic_fetch_add((LAS int*)post_addr(lds, C_OCNT, ln_), 1, __ATOMIC_RELAXED, __HIP_MEMORY_SCOPE_WORKGROUP)) == 5)
      st16(rs, ln_ < 8 ? WS_F + (unsigned)wg * 128 + ln_ * 16 : WS_BYTES, u32x4{1u, 1u, 1u, 1u});
    SBAR();
    op_step(F{}, T{}, F{}, wf[1], of[0], 0, wf[0], 0u, of[0]);
    op_step(T{}, T{}, F{}, wf[0], of[0], 1, wf[1], 0u, of[0]);
    int hd[8]; unsigned src[8];
#pragma unroll
    for (int m = 1; m < 8; ++m) { hd[m] = unit_head(m, h8, b); src[m] = o_in + (unsigned)hd[m] * 24576; asm volatile("" : "+s"(hd[m]), "+s"(src[m])); }
    unsigned rdy = 0;
    auto need = [&](int m) { if (!((rdy >> hd[m]) & 1)) rdy = (unsigned)spin_bit(lds + C_OFL, hd[m]); CFENCE(); };
#pragma unroll
    for (int m = 1; m <= 2; ++m) {
      need(m);
#pragma unroll
      for (int ks = 0; ks < 4; ++ks) of[m][ks] = ld16(rs, ln_ * 16 + ks * 1024, src[m]);
      SBAR();
      if (m == 1) STAMP(46);
    }
#pragma unroll
    for (int c = 1; c < 8; ++c) {
      if (c < 6) { need(c + 2); op_step(T{}, T{}, T{}, wf[c & 1], of[c], c + 1, wf[(c + 1) & 1], src[(c + 2) & 7], of[(c + 2) & 7]); }
      else if (c < 7) op_step(T{}, T{}, F{}, wf[c & 1], of[c], c + 1, wf[(c + 1) & 1], 0u, of[0]);
      else op_step(T{}, F{}, F{}, wf[1], of[7], 0, wf[0], 0u, of[0]);
    }
  }
  STAMP(49);
  {
    LAS char* xo = lds + EXTRA + widx * 4096 + ln_ * 16;
    LAS char* po = lds + EXTRA + (widx ^ 1) * 4096 + ln_ * 16;
#pragma unroll
    for (int r4 = 0; r4 < 4; ++r4) *(LAS f32x4*)(xo + r4 * 1024) = f32x4{acc[1][4 * r4], acc[1][4 * r4 + 1], acc[1][4 * r4 + 2], acc[1][4 * r4 + 3]};
    LGKM0();
    lds_post(lds, C_RED + 4 * wave, 1, ln_);
    spin_gt(lds + C_RED + 4 * partner, 0);
    const unsigned p_out = WS_P + (unsigned)((b * NH + (r32_ >> 3) * 8 + h8) * 24 + oct * 6 + widx) * 1024 + hh_ * 128 + (r32_ & 7) * 16;
    f32x4 t[4];
#pragma unroll
    for (int j = 0; j < 4; ++j) t[j] = *(LAS f32x4*)(po + j * 1024);
#pragma unroll
    for (int j = 0; j < 4; ++j) {
      const f32x4 v = {acc[0][4 * j] + t[j][0], acc[0][4 * j + 1] + t[j][1], acc[0][4 * j + 2] + t[j][2], acc[0][4 * j + 3] + t[j][3]};
      st16(rs, p_out + j * 256, __builtin_bit_cast(u32x4, v));
    }
    STAMP(50);
    VMW(0);
    st16(rs, ln_ < 8 ? WS_G + (unsigned)(wg * 8 + widx) * 128 + ln_ * 16 : WS_BYTES, u32x4{1u, 1u, 1u, 1u});
    VMW(0);
#pragma unroll 1
    for (int spin = 0; spin < SPIN_MAX; ++spin) {
      CFENCE();
      if (__all(ld4(rs, (ln_ & 3) * 8192 + (ln_ & 4) * 32, WS_G + (unsigned)((b * NH + h8) * 8 + 2 * rb) * 128) == 1u)) break;
    }
    CFENCE();
    STAMP(41);
    u32x4 pv[4];
#pragma unroll
    for (int j = 0; j < 4; ++j) pv[j] = ld16(rs, (ln_ >> 2) * 128 + (ln_ & 3) * 16, WS_P + (unsigned)(wg * 24 + j * 6 + 2 * rb) * 1024 + kh * 64);
    const f32x4 s = (__builtin_bit_cast(f32x4, pv[0]) + __builtin_bit_cast(f32x4, pv[1])) + (__builtin_bit_cast(f32x4, pv[2]) + __builtin_bit_cast(f32x4, pv[3]));
    *(u32x2*)(out + ((size_t)b * SQ + 32 * rb + 8 * oct + 4 * kh + (ln_ & 3)) * DM + 64 * h8 + 4 * (ln_ >> 2)) = u32x2{cvtpk(s[0], s[1]), cvtpk(s[2], s[3])};
  }
  STAMP(51);
}

extern "C" void kernel_launch(void* const* d_in, const int* in_sizes, int n_in, void* d_out, int out_size, void* d_ws, size_t ws_size,
                              hipStream_t stream) {
  (void)hipFuncSetAttribute((const void*)fused_kernel, hipFuncAttributeMaxDynamicSharedMemorySize, LDS_BYTES);
  hipLaunchKernelGGL(fused_kernel, dim3(256), dim3(512), LDS_BYTES, stream, (const __bf16*)d_in[0], (const __bf16*)d_in[1], (const __bf16*)d_in[2],
                     (const __bf16*)d_in[3], (const __bf16*)d_in[4], (__bf16*)d_out, (char*)d_ws);
}
```

```cpp
#include <hip/hip_runtime.h>
#include <hip/hip_bf16.h>
#include <stdint.h>
#include <type_traits>

#define LAS __attribute__((address_space(3)))
#define GAS __attribute__((address_space(1)))
#define DI __device__ __forceinline__
typedef __attribute__((ext_vector_type(4))) __bf16 bf16x4;
typedef __attribute__((ext_vector_type(8))) __bf16 bf16x8;
typedef __attribute__((ext_vector_type(2))) __bf16 bf16x2;
typedef __attribute__((ext_vector_type(4))) float f32x4;
typedef __attribute__((ext_vector_type(16))) float f32x16;
typedef __attribute__((ext_vector_type(2))) float f32x2;
typedef __attribute__((ext_vector_type(4))) unsigned u32x4;
typedef __attribute__((ext_vector_type(2))) unsigned u32x2;

constexpr int SQ = 96, DM = 512, NH = 32, SK = 1024, DH = 128;
constexpr int SLOT = 16384;
constexpr int KRING = 0, VRING = 4 * SLOT, EXTRA = 8 * SLOT;
constexpr int CTRL = EXTRA + 28672;
constexpr int C_WX = CTRL, C_KV = CTRL + 8, C_WL = CTRL + 16, C_WL2 = CTRL + 24, C_DONE = CTRL + 32, C_QRDY = CTRL + 64, C_MRG = CTRL + 96,
              C_RED = CTRL + 128, C_CLR = CTRL + 160, C_PEERS = CTRL + 192, C_OFL = CTRL + 200, C_OCNT = CTRL + 208, C_OISS = CTRL + 212,
              C_DUMMY = CTRL + 256;
constexpr int LDS_BYTES = 163840;
constexpr float SCL = 0.08838834764831845f * 1.4426950408889634f;
constexpr float PSUM_MAX = 4096.0f;
constexpr unsigned WBUF = 0x65840658u, XBUF = 0x73921739u;
constexpr unsigned OSLOT = 0x76210543u;
constexpr unsigned WS_O = 0, WS_P = 8u << 20, WS_H = 15u << 20, WS_F = WS_H + (64u << 10), WS_G = WS_F + (256u << 10), WS_BYTES = 16u << 20;
constexpr int SPIN_MAX = 1 << 16;
#define TAU 56

DI unsigned cvtpk(float lo, float hi) { f32x2 v = {lo, hi}; bf16x2 b = __builtin_convertvector(v, bf16x2); return __builtin_bit_cast(unsigned, b); }
DI f32x4 mfma16(bf16x8 a, bf16x8 b, f32x4 c) { return __builtin_amdgcn_mfma_f32_16x16x32_bf16(a, b, c, 0, 0, 0); }
DI f32x16 mfma32(bf16x8 a, bf16x8 b, f32x16 c) { return __builtin_amdgcn_mfma_f32_32x32x16_bf16(a, b, c, 0, 0, 0); }
DI bf16x4 trread(LAS char* p) { return __builtin_amdgcn_ds_read_tr16_b64_v4bf16((LAS bf16x4*)p); }
DI bf16x8 cat8(bf16x4 a, bf16x4 b) { return __builtin_shufflevector(a, b, 0, 1, 2, 3, 4, 5, 6, 7); }
DI bf16x8 asbf(const u32x4& w) { return __builtin_bit_cast(bf16x8, w); }
DI float xmax2(float v) {
  auto s = __builtin_amdgcn_permlane32_swap(__float_as_uint(v), __float_as_uint(v), false, false);
  return fmaxf(__uint_as_float(s[0]), __uint_as_float(s[1]));
}
DI u32x4 exp8(const f32x16& s, int o) {
  u32x4 w;
#pragma unroll
  for (int i = 0; i < 4; ++i) w[i] = cvtpk(__builtin_amdgcn_exp2f(s[o + 2 * i]), __builtin_amdgcn_exp2f(s[o + 2 * i + 1]));
  return w;
}
#define CFENCE() asm volatile("" ::: "memory")
#define LGKM0() do { CFENCE(); __builtin_amdgcn_s_waitcnt(0xc07f); CFENCE(); } while (0)
#define VMW(N) asm volatile("s_waitcnt vmcnt(" #N ")" ::: "memory")
#define SBAR() __builtin_amdgcn_sched_barrier(0)
#define PIN(x) asm volatile("" : "+v"(x))
DI int rfl(int v) { return __builtin_amdgcn_readfirstlane(v); }
DI int lds_word(LAS char* p) { return rfl(*(volatile LAS int*)p); }
DI int lds_min2(LAS char* p) { u32x2 v = *(volatile LAS u32x2*)p; return rfl(min((int)v[0], (int)v[1])); }
DI int lds_min6(LAS char* p) {
  u32x4 v = *(volatile LAS u32x4*)p; u32x2 w = *(volatile LAS u32x2*)(p + 16);
  return rfl(min(min(min((int)v[0], (int)v[1]), min((int)v[2], (int)v[3])), min((int)w[0], (int)w[1])));
}
DI LAS char* post_addr(LAS char* lds, int off, int lane) { return lds + (lane == 0 ? off : C_DUMMY + 4 * lane); }
DI void lds_post(LAS char* lds, int off, int v, int lane) { *(volatile LAS int*)post_addr(lds, off, lane) = v; }
DI void lds_bump(LAS char* p) { __hip_atomic_fetch_add((LAS int*)p, 1, __ATOMIC_RELAXED, __HIP_MEMORY_SCOPE_WORKGROUP); }
typedef __amdgpu_buffer_rsrc_t rsrc_t;
DI void st16(rsrc_t r, unsigned off, u32x4 v) { __builtin_amdgcn_raw_buffer_store_b128(v, r, (int)off, 0, 16); }
DI u32x4 ld16(rsrc_t r, unsigned voff, unsigned soff) { return __builtin_amdgcn_raw_buffer_load_b128(r, (int)voff, (int)soff, 16); }
DI void st4(rsrc_t r, unsigned off, unsigned v) { __builtin_amdgcn_raw_buffer_store_b32(v, r, (int)off, 0, 16); }
DI unsigned ld4(rsrc_t r, unsigned voff, unsigned soff) { return __builtin_amdgcn_raw_buffer_load_b32(r, (int)voff, (int)soff, 16); }
DI int fresh_lane() { int l; asm volatile("v_mbcnt_lo_u32_b32 %0, -1, 0\n\tv_mbcnt_hi_u32_b32 %0, -1, %0" : "=v"(l)); return l; }
DI int unit_head(int pos, int h8, int b) { int n = pos - 1 + 2 * (b & 3); n = n >= 7 ? n - 7 : n; return pos == 0 ? h8 : ((h8 + 1 + n) & 7); }

#define STAMP(i) do {} while (0)

DI int spin_gt(LAS char* p, int n) {
  int v, r;
  asm volatile("1:\n\tds_read_b32 %1, %2\n\ts_waitcnt lgkmcnt(0)\n\tv_readfirstlane_b32 %0, %1\n\ts_cmp_le_i32 %0, %3\n\ts_cbranch_scc1 1b"
               : "=&s"(r), "=&v"(v) : "v"(p), "s"(n) : "memory", "scc");
  return r;
}
DI void spin_all_gt(LAS char* p, int n) {
  int v;
  asm volatile("1:\n\tds_read_b32 %0, %1\n\ts_waitcnt lgkmcnt(0)\n\tv_cmp_ge_i32 vcc, %2, %0\n\ts_cbranch_vccnz 1b"
               : "=&v"(v) : "v"(p), "v"(n) : "memory", "vcc");
}
DI int spin_bit(LAS char* p, int bit) {
  int v, r;
  asm volatile("1:\n\tds_read_b32 %1, %2\n\ts_waitcnt lgkmcnt(0)\n\tv_readfirstlane_b32 %0, %1\n\ts_bitcmp0_b32 %0, %3\n\ts_cbranch_scc1 1b"
               : "=&s"(r), "=&v"(v) : "v"(p), "s"(bit) : "memory", "scc");
  return r;
}
DI void wait_gt(int& have, int n, LAS char* p) {
  while (have <= n) { have = lds_word(p); if (have <= n) __builtin_amdgcn_s_sleep(1); }
  CFENCE();
}

struct Attn {
  f32x16 o[4];
  f32x16 negm;
  bf16x8 qf[8], kf[8];
  bf16x4 vf[16];
  u32x4 pf[2];
  bf16x8 ones;
  float lsum;
  int have;
  LAS char *kb[8], *vb[4], *land, *donep;

  DI void release() { LGKM0(); lds_bump(donep); }
  template <int S> DI void kread(int t) { kf[t] = *(LAS bf16x8*)(kb[t] + S * SLOT); }
  template <int S> DI void vread(int i) { vf[i] = trread(vb[(i >> 1) & 3] + S * SLOT + (i >> 3) * 4096 + (i & 1) * 2048); }
  DI bf16x8 vfrag(int m) { return cat8(vf[2 * m], vf[2 * m + 1]); }
  DI float rowmax(const f32x16& s) {
    float mx = s[0];
#pragma unroll
    for (int r = 1; r < 16; ++r) mx = fmaxf(mx, s[r]);
    return xmax2(mx);
  }
  DI f32x4 psum(const u32x4& p0, const u32x4& p1) { return mfma16(ones, asbf(p1), mfma16(ones, asbf(p0), f32x4{0.f, 0.f, 0.f, 0.f})); }
  template <int S> DI void tile(f32x16& cur, f32x16& nxt, int i) {
    release(); wait_gt(have, 2 * i + 2, land);
    SBAR();
    f32x4 ts = {0.f, 0.f, 0.f, 0.f};
    float ea, eb, pa = 0.f, pb = 0.f;
#pragma unroll
    for (int m = 0; m < 8; ++m) {
      nxt = mfma32(kf[m], qf[m], m == 0 ? negm : nxt);
      if (m < 5) { vread<S>(3 * m); vread<S>(3 * m + 1); vread<S>(3 * m + 2); }
      if (m == 5) vread<S>(15);
      ea = __builtin_amdgcn_exp2f(cur[2 * m]); eb = __builtin_amdgcn_exp2f(cur[2 * m + 1]);
      asm volatile("" : "+v"(ea), "+v"(eb));
      if (m > 0) { unsigned w = cvtpk(pa, pb); PIN(w); pf[(m - 1) >> 2][(m - 1) & 3] = w; }
      pa = ea; pb = eb;
      if (m == 6) ts = mfma16(ones, asbf(pf[0]), ts);
      SBAR();
    }
    pf[1][3] = cvtpk(pa, pb);
    ts = mfma16(ones, asbf(pf[1]), ts);
    release(); wait_gt(have, 2 * i + 3, land);
    if (__builtin_expect(__any(!(ts[0] <= PSUM_MAX)), 0)) {
      const float dl = fmaxf(rowmax(cur), 0.f);
      const float al = __builtin_amdgcn_exp2f(-dl);
#pragma unroll
      for (int r = 0; r < 16; ++r) { cur[r] -= dl; nxt[r] -= dl; negm[r] -= dl; }
      PIN(negm);
      pf[0] = exp8(cur, 0); pf[1] = exp8(cur, 8);
      ts = psum(pf[0], pf[1]);
      lsum *= al;
#pragma unroll
      for (int c = 0; c < 4; ++c) o[c] *= al;
      PIN(lsum); PIN(ts); PIN(nxt); PIN(pf[0]); PIN(pf[1]);
    }
    PIN(lsum); PIN(ts);
    lsum += ts[0];
    SBAR();
#pragma unroll
    for (int m = 0; m < 8; ++m) {
      o[m & 3] = mfma32(vfrag(m), asbf(pf[m >> 2]), o[m & 3]);
      if (m < 4) { kread<(S + 2) & 3>(2 * m); kread<(S + 2) & 3>(2 * m + 1); }
      SBAR();
    }
  }
};

__global__ __launch_bounds__(512, 2) void fused_kernel(const __bf16* __restrict__ x, const __bf16* __restrict__ kc, const __bf16* __restrict__ vc,
                                                       const __bf16* __restrict__ wq, const __bf16* __restrict__ wo, __bf16* __restrict__ out, char* ws) {
  extern __shared__ __attribute__((aligned(16))) char smem[];
  LAS char* lds = (LAS char*)smem;
  const int tid = threadIdx.x, lane = tid & 63, wave = (0x54321076u >> (4 * rfl(tid >> 6))) & 15;
  const int bid = blockIdx.x, xcd = bid & 7, idx = bid >> 3;
  const int oct = xcd & 3, h8 = idx & 7;
  const int h = oct * 8 + h8, b = (xcd >> 2) * 4 + (idx >> 3);
  const int wg = b * NH + h;
  using T = std::true_type; using F = std::false_type;
  STAMP(0);

  if (__builtin_expect(wave >= 6, 1)) {
    __builtin_amdgcn_s_setprio(3);
    const int L = wave - 6;
    const int r16 = lane & 15, g = lane >> 4;
    const char* wp = (const char*)(wq + h * DH) + (size_t)L * 32 * 8192;
    const char* xp = (const char*)(x + ((size_t)b * SQ + 48 * L) * DM);
    unsigned oa[4], ob[4];
#pragma unroll
    for (int i = 0; i < 4; ++i) {
      oa[i] = i * (32768 - 1024) + g * 8192 + ((r16 ^ (g << 2)) << 4);
      const int row = 8 * i + (lane >> 3);
      ob[i] = row * 1024 - i * 1024 + (((lane & 7) ^ ((row >> 1) & 7)) << 4);
    }
    LAS char* lb = lds + L * 8192;
    int seen = 0;
#define GLDS(src, dst, off, aux) __builtin_amdgcn_global_load_lds((const GAS void*)(src), (LAS void*)(dst), 16, off, aux)
#define GLDS4(src, o0, o1, o2, o3, dst, aux) do { GLDS((src) + (o0), dst, 0, aux); GLDS((src) + (o1), dst, 1024, aux); GLDS((src) + (o2), dst, 2048, aux); GLDS((src) + (o3), dst, 3072, aux); } while (0)
#define POST(p, v) (*(volatile LAS int*)(p) = (v))
    auto wait_done = [&](int need) {
      while (seen < need) { seen = lds_min6(lds + C_DONE); if (seen < need) __builtin_amdgcn_s_sleep(1); }
      CFENCE();
    };
    LAS char* pw = post_addr(lds, C_WX + 4 * L, lane);
#pragma unroll 1
    for (int cc = 0; cc < 8; ++cc) {
      wait_done(cc - 4);
      LAS char* xd = lds + ((XBUF >> (4 * cc)) & 15) * SLOT + L * 6144;
      LAS char* wd = lb + ((WBUF >> (4 * cc)) & 15) * SLOT;
      const char* xs = xp + cc * 128;
      GLDS4(xs, ob[0], ob[1], ob[2], ob[3], xd, 0);
      GLDS(xs + 32768 + ob[0], xd + 4096, 0, 0); GLDS(xs + 32768 + ob[1], xd + 4096, 1024, 0);
      if (cc >= 2) { VMW(20); POST(pw, cc - 1); }
      const char* wsrc = wp + (size_t)cc * 64 * 8192;
      GLDS4(wsrc, oa[0], oa[1], oa[2], oa[3], wd, 0);
      GLDS4(wsrc + 16 * 8192, oa[0], oa[1], oa[2], oa[3], wd + 4096, 0);
      STAMP(1 + cc);
      if (cc == 0) asm volatile("s_barrier" ::: "memory");
    }
    const char* kp = (const char*)(kc + (size_t)wg * SK * DH) + L * 8192;
    const char* vp = (const char*)(vc + (size_t)wg * SK * DH) + L * 8192;
    const unsigned ov = g * 256 + ((r16 ^ (g << 2)) << 4);
#pragma unroll
    for (int i = 0; i < 4; ++i) {
      oa[i] = g * 256 + ((r16 ^ (4 * i + g)) << 4);
      const int row = 8 * i + (lane >> 3);
      ob[i] = row * 1024 - i * 1024 + (((lane & 7) ^ (((row >> 1) & 1) << 2)) << 4);
    }
    auto issue_k = [&](int n) {
      const char* src = kp + n * SLOT; LAS char* dst = lb + KRING + (n & 3) * SLOT;
      GLDS4(src, oa[0], oa[1], oa[2], oa[3], dst, 2); GLDS4(src + 4096, oa[0], oa[1], oa[2], oa[3], dst + 4096, 2);
    };
    auto issue_v = [&](int n) {
      const char* src = vp + n * SLOT; LAS char* dst = lb + VRING + (n & 3) * SLOT;
      GLDS4(src, ov, ov, ov, ov, dst, 2); GLDS4(src + 4096, ov, ov, ov, ov, dst + 4096, 2);
    };
    wait_done(4);
    unsigned due = (unsigned)__builtin_amdgcn_s_memrealtime() + 2 * TAU;
    issue_k(0);
    STAMP(17);
    VMW(22); POST(pw, 7);
    issue_k(1);
    VMW(16); POST(pw, 8);
    LAS char* pk = post_addr(lds, C_KV + 4 * L, lane);
    auto gate = [&](int j, int need) {
      if (seen < need) {
        seen = lds_min6(lds + C_DONE);
        if (seen < need) {
          VMW(0); POST(pk, j);
          do { __builtin_amdgcn_s_sleep(1); seen = lds_min6(lds + C_DONE); } while (seen < need);
        }
      }
      while ((int)((unsigned)__builtin_amdgcn_s_memrealtime() - due) < 0) __builtin_amdgcn_s_sleep(1);
      due += TAU;
      CFENCE();
    };
#define PUBLISH(j) do { VMW(8); POST(pk, j); } while (0)
#pragma unroll 1
    for (int n = 0; n < 12; ++n) {
      const int j = 2 * n + 2;
      gate(j, n < 4 ? ((0x9875 >> (4 * n)) & 15) : j + 1);
      issue_v(n); PUBLISH(j);
      gate(j + 1, n < 3 ? ((0x975 >> (4 * n)) & 15) : j + 2);
      issue_k(n + 2); PUBLISH(j + 1);
      STAMP(18 + n);
    }
    gate(26, 28); issue_k(14); PUBLISH(26);
    gate(27, 30); issue_k(15); PUBLISH(27);
    gate(28, 30); issue_v(12); PUBLISH(28);
    gate(29, 30); issue_v(13); PUBLISH(29);
    gate(30, 31); issue_v(14); PUBLISH(30);
    gate(31, 33); issue_v(15);
    STAMP(48);
    PUBLISH(31);
    const char* op = (const char*)(wo + (size_t)(oct * 1024 + 64 * L) * DM + h8 * 64);
    auto issue_o = [&](int pos) {
      const char* src = op + (size_t)unit_head(pos, h8, b) * 128 * 1024; LAS char* dst = lb + ((OSLOT >> (4 * pos)) & 15) * SLOT;
      GLDS4(src, ob[0], ob[1], ob[2], ob[3], dst, 0); GLDS4(src + 32768, ob[0], ob[1], ob[2], ob[3], dst + 4096, 0);
    };
    LAS char* pl = post_addr(lds, C_WL + 4 * L, lane);
    __builtin_amdgcn_s_setprio(0);
    wait_done(36);
    issue_o(0);
    PUBLISH(32);
    STAMP(49);
    wait_done(37); issue_o(1);
    while (lds_word(lds + C_OISS) < 6) __builtin_amdgcn_s_sleep(1);
    wait_done(41);
    issue_o(2);
    VMW(8); POST(pl, 2);
    if (L == 1) {
      LAS char* pl2 = post_addr(lds, C_WL2, lane);
#pragma unroll 1
      for (int pos = 3; pos < 8; ++pos) {
        issue_o(pos);
        op -= 65536; lb -= 8192; issue_o(pos); op += 65536; lb += 8192;
        VMW(16); POST(pos == 3 ? pl : pl2, pos);
      }
      VMW(0); POST(pl2, 8);
    } else {
      VMW(0); POST(pl, 3);
      const rsrc_t rs = __builtin_amdgcn_make_buffer_rsrc(ws, 0, WS_BYTES, 0x00020000);
      if (lds_word(lds + C_PEERS) == 0) {
        const bool peer = lane < 32 && ((lane >> 3) == oct || (lane & 7) == h8);
        const unsigned hello_to = peer ? WS_H + (unsigned)(b * NH + lane) * 128 + h * 4 : WS_BYTES;
        for (int spin = 0; spin < SPIN_MAX; ++spin) {
          st4(rs, hello_to, 1u); __builtin_amdgcn_s_sleep(8);
          if (!__any(peer && ld4(rs, (lane & 31) * 4, WS_H + (unsigned)wg * 128) != 1u)) break;
        }
        st4(rs, hello_to, 1u);
      }
      while (lds_word(lds + C_OCNT) < 6) __builtin_amdgcn_s_sleep(2);
      LAS char* pm = post_addr(lds, C_OFL, lane);
      for (int spin = 0; spin < SPIN_MAX; ++spin) {
        CFENCE();
        const unsigned m = (unsigned)__builtin_amdgcn_ballot_w64(ld4(rs, (lane & 7) * 128, WS_F + (unsigned)(b * NH + oct * 8) * 128) == 1u) & 0xffu;
        POST(pm, spin == SPIN_MAX - 1 ? 0xff : (int)m);
        if (m == 0xffu) break;
        __builtin_amdgcn_s_sleep(2);
      }
    }
    STAMP(58);
    return;
  }
  const int kh = wave >= 3, rb = wave < 3 ? wave : (wave == 3 ? 2 : wave - 4);
  const int partner = rb == 2 ? (wave ^ 1) : (wave ^ 4);
  const int widx = 2 * rb + kh;
  if (wave == 0) *(LAS int*)(lds + CTRL + 4 * lane) = 0;
  asm volatile("s_waitcnt lgkmcnt(0)\n\ts_barrier" ::: "memory");
  const int r32 = lane & 31, hh = lane >> 5, blk = (lane >> 4) & 1, qq = (lane & 15) >> 2, p4 = lane & 3;
  const rsrc_t rs = __builtin_amdgcn_make_buffer_rsrc(ws, 0, WS_BYTES, 0x00020000);
  const unsigned o_out = WS_O + (unsigned)((wg * 3 + rb) * 8 + 4 * kh) * 1024;
  Attn st;
  st.donep = post_addr(lds, C_DONE + 4 * wave, lane);
  {
    f32x16 qa[2] = {f32x16{}, f32x16{}};
    const int wadr = (8 * hh + qq) * 256 + (((4 * kh + blk) ^ (qq << 1)) << 5) + p4 * 8;
    const int xrow = 32 * rb + r32;
    const int xadr = xrow * 128 + ((hh ^ ((xrow >> 1) & 7)) << 4);
    bf16x8 xa[4], xb[4];
    bf16x4 wa[16], wb[16];
    LAS char* wxp = lds + C_WX + 4 * (lane & 1);
    auto qp_step = [&](auto MMA, auto RD, bf16x8 (&xc)[4], bf16x4 (&wc)[16], int cn, bf16x8 (&xn)[4], bf16x4 (&wn)[16]) {
      LAS char* xb_ = lds + ((XBUF >> (4 * cn)) & 15) * SLOT;
      LAS char* wb_ = lds + ((WBUF >> (4 * cn)) & 15) * SLOT;
      int fl = 0;
#pragma unroll
      for (int m = 0; m < 8; ++m) {
        if constexpr (MMA.value) qa[m & 1] = mfma32(cat8(wc[2 * m], wc[2 * m + 1]), xc[m >> 1], qa[m & 1]);
        if constexpr (RD.value) {
          constexpr int lo[7] = {0, 4, 8, 11, 14, 17, 20};
          if (m < 6) {
#pragma unroll
            for (int i = lo[m]; i < lo[m + 1]; ++i) {
              const int ks = i / 5, e = i % 5 - 1;
              if (e < 0) xn[ks] = *(LAS bf16x8*)(xb_ + (xadr ^ (ks << 5)));
              else wn[4 * ks + e] = trread(wb_ + (wadr ^ ((e >> 1) << 6)) + ks * 4096 + (e & 1) * 1024);
            }
          }
          if (m == 5) fl = *(volatile LAS int*)wxp;
        }
        SBAR();
      }
      if constexpr (RD.value) {
        st.release();
        if (cn < 7 && __any(fl <= cn + 1)) spin_all_gt(wxp, cn + 1);
        CFENCE();
      }
    };
    while (lds_min2(lds + C_WX) < 1) __builtin_amdgcn_s_sleep(1);
    CFENCE();
    STAMP(1);
    qp_step(F{}, T{}, xb, wb, 0, xa, wa);
#pragma unroll 1
    for (int cc = 0; cc < 6; cc += 2) {
      qp_step(T{}, T{}, xa, wa, cc + 1, xb, wb);
      qp_step(T{}, T{}, xb, wb, cc + 2, xa, wa);
    }
    STAMP(8);
    qp_step(T{}, T{}, xa, wa, 7, xb, wb);
    qp_step(T{}, F{}, xb, wb, 0, xa, wa);
    spin_all_gt(lds + C_DONE + 4 * min(lane, 5), 5);
    LAS char* ql = lds + EXTRA + xrow * 256;
#pragma unroll
    for (int i = 0; i < 2; ++i)
#pragma unroll
      for (int r4 = 0; r4 < 4; ++r4) {
        const int ch = 4 * (2 * kh + i) + r4;
        u32x2 w = {cvtpk(qa[i][4 * r4] * SCL, qa[i][4 * r4 + 1] * SCL), cvtpk(qa[i][4 * r4 + 2] * SCL, qa[i][4 * r4 + 3] * SCL)};
        *(LAS u32x2*)(ql + ((ch ^ (r32 & 15)) << 4) + hh * 8) = w;
      }
    LGKM0();
    lds_post(lds, C_QRDY + 4 * wave, 1, lane);
    spin_all_gt(lds + (lane ? C_KV + 4 * kh : C_QRDY + 4 * partner), lane ? 1 : 0);
#pragma unroll
    for (int t = 0; t < 8; ++t) st.qf[t] = *(LAS bf16x8*)(ql + (((2 * t + hh) ^ (r32 & 15)) << 4));
  }
  STAMP(9);
  {
    const int kadr = KRING + kh * 8192 + r32 * 256 + ((hh ^ (r32 & 15)) << 4);
    const int vadr = VRING + kh * 8192 + (4 * hh + qq) * 256 + ((blk ^ (qq << 1)) << 5) + p4 * 8;
#pragma unroll
    for (int t = 0; t < 8; ++t) st.kb[t] = lds + (kadr ^ (t << 5));
#pragma unroll
    for (int c = 0; c < 4; ++c) st.vb[c] = lds + (vadr ^ ((c ^ (2 * kh)) << 6));
    const unsigned one = (blk == (qq & 1)) ? 0x3f803f80u : 0u;
    st.ones = asbf(u32x4{one, one, one, one});
  }
  st.land = lds + C_KV + 4 * kh;
#pragma unroll
  for (int c = 0; c < 4; ++c) st.o[c] = f32x16{};
  st.lsum = 0.f; st.have = 0;
  f32x16 sA, sB;
  {
#pragma unroll
    for (int t = 0; t < 8; ++t) st.kread<0>(t);
    SBAR();
    sA = f32x16{};
#pragma unroll
    for (int t = 0; t < 8; ++t) sA = mfma32(st.kf[t], st.qf[t], sA);
    SBAR();
    st.release();
#pragma unroll
    for (int t = 0; t < 8; ++t) st.kread<1>(t);
    SBAR();
    const float mx = st.rowmax(sA);
#pragma unroll
    for (int r = 0; r < 16; ++r) { sA[r] -= mx; st.negm[r] = -mx; }
  }
  auto hello = [&]() {
    const int ln = fresh_lane();
    const bool peer = ln < 32 && ((ln >> 3) == oct || (ln & 7) == h8);
    st4(rs, peer ? WS_H + (unsigned)(b * NH + ln) * 128 + h * 4 : WS_BYTES, 1u);
  };
  STAMP(10);
#pragma unroll 1
  for (int it = 0; it < 12; it += 4) {
    st.tile<0>(sA, sB, it); st.tile<1>(sB, sA, it + 1); st.tile<2>(sA, sB, it + 2); st.tile<3>(sB, sA, it + 3);
    if (wave == 2) {
      if (it == 0) {
        const int ln = fresh_lane();
        st16(rs, ln < 48 ? WS_G + (unsigned)wg * 1024 + ln * 16 : (ln < 56 ? WS_F : WS_H) + (unsigned)wg * 128 + (ln & 7) * 16, u32x4{0u, 0u, 0u, 0u});
      } else if (it == 4) {
        VMW(0);
        hello();
      }
    }
    if (wave == 3 && it == 4) {
      const int ln = fresh_lane();
      const char* p = (const char*)(wo + (size_t)(oct * 1024 + (ln & 1) * 1023) * DM);
      if (ln == 4) p = (const char*)(out + (size_t)b * SQ * DM);
      unsigned w0 = __builtin_nontemporal_load((const unsigned*)p), w1 = ld4(rs, (ln & 7) * (2u << 20), 0);
      asm volatile("" :: "v"(w0), "v"(w1));
    }
    STAMP(11 + (it >> 2));
  }
  const int ln_ = fresh_lane();
  const int r32_ = ln_ & 31, hh_ = ln_ >> 5, blk_ = (ln_ >> 4) & 1, qq_ = (ln_ & 15) >> 2, p4_ = ln_ & 3;
  u32x4 of[8][4];
  {
    unsigned hin = 0;
    if (wave == 2) hin = ld4(rs, (ln_ & 31) * 4, WS_H + (unsigned)wg * 128);
    u32x4 pq[8];
    auto sm_step = [&](auto GG, f32x16& cur, f32x16& nxt) {
      constexpr int G = GG.value;
      if constexpr (G < 3) st.release();
      SBAR();
#pragma unroll
      for (int t = 0; t < 8; ++t) {
        if constexpr (G < 3) nxt = mfma32(st.kf[t], st.qf[t], t == 0 ? st.negm : nxt);
        unsigned w = cvtpk(__builtin_amdgcn_exp2f(cur[2 * t]), __builtin_amdgcn_exp2f(cur[2 * t + 1]));
        PIN(w); pq[2 * G + (t >> 2)][t & 3] = w;
        SBAR();
      }
      f32x4 ts = st.psum(pq[2 * G], pq[2 * G + 1]);
      if constexpr (G < 2) {
        wait_gt(st.have, 26 + G, st.land);
#pragma unroll
        for (int t = 0; t < 8; ++t) st.kread<2 + G>(t);
      }
      SBAR();
      if (__builtin_expect(__any(!(ts[0] <= PSUM_MAX)), 0)) {
        const float dl = __builtin_ceilf(fmaxf(st.rowmax(cur), 0.f));
        const float al = __builtin_amdgcn_exp2f(-dl);
#pragma unroll
        for (int r = 0; r < 16; ++r) { cur[r] -= dl; if (G < 3) nxt[r] -= dl; st.negm[r] -= dl; }
        PIN(st.negm);
        pq[2 * G] = exp8(cur, 0); pq[2 * G + 1] = exp8(cur, 8);
        ts = st.psum(pq[2 * G], pq[2 * G + 1]);
        st.lsum *= al;
#pragma unroll
        for (int c = 0; c < 4; ++c) st.o[c] *= al;
#pragma unroll
        for (int i = 0; i < 8 * G; ++i) { const unsigned w = pq[i >> 2][i & 3]; pq[i >> 2][i & 3] = cvtpk(__uint_as_float(w << 16) * al, __uint_as_float(w & 0xffff0000u) * al); }
        PIN(st.lsum); PIN(ts); if (G < 3) PIN(nxt);
#pragma unroll
        for (int i = 0; i < 2 * G + 2; ++i) PIN(pq[i]);
      }
      PIN(st.lsum); PIN(ts);
      st.lsum += ts[0];
    };
    sm_step(std::integral_constant<int, 0>{}, sA, sB);
    sm_step(std::integral_constant<int, 1>{}, sB, sA);
    if (wave == 2) {
      if (!__any(ln_ < 32 && ((ln_ >> 3) == oct || (ln_ & 7) == h8) && hin != 1u)) { hello(); lds_post(lds, C_PEERS, 1, ln_); }
    }
    sm_step(std::integral_constant<int, 2>{}, sA, sB);
    sm_step(std::integral_constant<int, 3>{}, sB, sA);
    const float lsum = st.lsum, mrun = -st.negm[0];
    {
      LAS char* xp_ = lds + EXTRA + widx * 4096 + ln_ * 16;
#pragma unroll
      for (int i = 0; i < 4; ++i) *(LAS u32x4*)(xp_ + i * 1024) = pq[4 + i];
      *(LAS f32x2*)(lds + EXTRA + 24576 + widx * 512 + ln_ * 8) = f32x2{mrun, lsum};
    }
    LGKM0();
    lds_post(lds, C_MRG + 4 * wave, 1, ln_);
    STAMP(39);
    wait_gt(st.have, 28, st.land);
#pragma unroll
    for (int j = 0; j < 16; ++j) st.vread<0>(j);
    SBAR();
#pragma unroll
    for (int m = 0; m < 8; ++m) st.o[m & 3] = mfma32(st.vfrag(m), asbf(pq[m >> 2]), st.o[m & 3]);
    st.release();
    SBAR();
    LAS char* mrgp = lds + C_MRG + 4 * partner;
    spin_all_gt(ln_ < 6 ? lds + C_DONE + 4 * ln_ : mrgp, ln_ < 6 ? 34 : 0);
    u32x4 pp[4];
    const f32x2 ml = *(LAS f32x2*)(lds + EXTRA + 24576 + (widx ^ 1) * 512 + ln_ * 8);
    {
      LAS char* pp_ = lds + EXTRA + (widx ^ 1) * 4096 + ln_ * 16;
#pragma unroll
      for (int i = 0; i < 4; ++i) pp[i] = *(LAS u32x4*)(pp_ + i * 1024);
    }
    if (st.have <= 29) spin_gt(st.land, 29);
#pragma unroll
    for (int j = 0; j < 16; ++j) st.vread<1>(j ^ 4);
    const float mn = fmaxf(mrun, ml[0]);
    const float a0 = __builtin_amdgcn_exp2f(mrun - mn), a1 = __builtin_amdgcn_exp2f(ml[0] - mn);
    const float inv = 1.0f / (a0 * lsum + a1 * ml[1]);
    float f0 = a0 * inv, f1 = a1 * inv;
    PIN(f0); PIN(f1);
    SBAR();
    LAS char* xo = lds + KRING + widx * 8192 + ln_ * 16;
#pragma unroll
    for (int m = 0; m < 8; ++m) {
      const int mm = m < 2 ? m + 2 : (m < 4 ? m + 4 : (m < 6 ? m - 4 : m - 2));
      st.o[mm & 3] = mfma32(st.vfrag(mm), asbf(pq[2 + (mm >> 2)]), st.o[mm & 3]);
      if (m >= 5) {
#pragma unroll
        for (int q = (m - 5) * 3; q < (m - 5) * 3 + 3 && q < 8; ++q) {
          const f32x16& v = st.o[2 + (q >> 2)]; const int r = 4 * (q & 3);
          *(LAS f32x4*)(xo + q * 1024) = f32x4{v[r], v[r + 1], v[r + 2], v[r + 3]};
        }
      }
      SBAR();
    }
    st.release();
    lds_post(lds, C_MRG + 4 * wave, 2, ln_);
    STAMP(42);
    st.vb[2] = st.vb[0] + (kh ? -8192 : 8192); st.vb[3] = st.vb[1] + (kh ? -8192 : 8192);
    LAS char* kvp = lds + C_KV + 4 * (ln_ & 1);
    LAS char* po = lds + KRING + (widx ^ 1) * 8192 + ln_ * 16;
    auto get_o = [&]() {
#pragma unroll
      for (int q = 0; q < 8; ++q) {
        const f32x4 t = *(volatile LAS f32x4*)(po + q * 1024);
#pragma unroll
        for (int r = 0; r < 4; ++r) st.o[2 + (q >> 2)][4 * (q & 3) + r] = t[r];
      }
    };
    spin_all_gt(kvp, 30);
#pragma unroll
    for (int j = 0; j < 16; ++j) st.vread<2>((j & 3) + ((j >> 2) & 1) * 8 + (j >> 3) * 4);
    const int fl = *(volatile LAS int*)mrgp;
    get_o();
    SBAR();
#pragma unroll
    for (int q = 0; q < 4; ++q) { const int m = (q & 1) + (q >> 1) * 4; st.o[m & 3] = mfma32(st.vfrag(m), asbf(pq[4 + (m >> 2)]), st.o[m & 3]); }
    SBAR();
    if (rfl(fl) < 2) { spin_gt(mrgp, 1); get_o(); }
    st.release(); lds_bump(st.donep);
#pragma unroll
    for (int q = 0; q < 4; ++q) { const int m = 2 + (q & 1) + (q >> 1) * 4; st.o[m & 3] = mfma32(st.vfrag(m), asbf(pp[m >> 2]), st.o[m & 3]); }
    SBAR();
    STAMP(43);
    spin_all_gt(kvp, 31);
#pragma unroll
    for (int j = 0; j < 16; ++j) { const int mm = ((j >> 1) & 3) * 2 + (j >> 3); st.vread<3>(2 * mm + (j & 1)); }
    SBAR();
    auto fin = [&](int i, int s) {
      u32x4 w;
#pragma unroll
      for (int e = 0; e < 4; ++e) { const int r = 8 * s + 2 * e; w[e] = cvtpk(st.o[i][r] * f0 + st.o[2 + i][r] * f1, st.o[i][r + 1] * f0 + st.o[2 + i][r + 1] * f1); }
      of[0][2 * i + s] = w; st16(rs, o_out + ln_ * 16 + (2 * i + s) * 1024, w);
    };
#pragma unroll
    for (int q = 0; q < 8; ++q) {
      const int m = 2 * (q & 3) + (q >> 2);
      st.o[m & 3] = mfma32(st.vfrag(m), asbf((m & 2) ? pp[2 + (m >> 2)] : pq[6 + (m >> 2)]), st.o[m & 3]);
      if (q == 7) { st.release(); fin(0, 0); fin(0, 1); }
      SBAR();
    }
    fin(1, 0); fin(1, 1);
  }
  STAMP(44);
  f32x16 acc[2] = {f32x16{}, f32x16{}};
  {
    const int tadr = kh * 8192 + (4 * hh_ + qq_) * 128 + (((qq_ >> 1) ^ kh) << 6) + blk_ * 32 + p4_ * 8;
    const unsigned o_in = WS_O + (unsigned)((b * NH + oct * 8) * 24 + rb * 8 + 4 * kh) * 1024;
    int whave = 0;
    bf16x4 wf[2][16];
    auto op_step = [&](auto MMA, auto RD, auto LD, bf16x4 (&wc)[16], u32x4 (&oc)[4], int pn, bf16x4 (&wn)[16], unsigned src, u32x4 (&on)[4]) {
      if constexpr (RD.value) { if (whave <= pn) whave = spin_gt(lds + (pn < 3 ? C_WL + 4 * kh : C_WL2), pn); }
      LAS char* wb_ = lds + ((OSLOT >> (4 * pn)) & 15) * SLOT;
#pragma unroll
      for (int m = 0; m < 8; ++m) {
        if constexpr (MMA.value) acc[m & 1] = mfma32(cat8(wc[2 * m], wc[2 * m + 1]), asbf(oc[m >> 1]), acc[m & 1]);
        if constexpr (RD.value) {
          if (m < 6) {
#pragma unroll
            for (int i = 3 * m; i < 3 * m + 3 && i < 16; ++i) wn[i] = trread(wb_ + (tadr ^ (((i >> 1) & 1) << 6)) + (i >> 2) * 2048 + (i & 1) * 1024);
          }
        }
        if constexpr (LD.value) { if (m & 1) on[m >> 1] = ld16(rs, ln_ * 16 + (m >> 1) * 1024, src); }
        SBAR();
      }
    };
    lds_bump(post_addr(lds, C_OISS, ln_));
    VMW(0);
    STAMP(40);
    if (rfl(__hip_atomic_fetch_add((LAS int*)post_addr(lds, C_OCNT, ln_), 1, __ATOMIC_RELAXED, __HIP_MEMORY_SCOPE_WORKGROUP)) == 5)
      st16(rs, ln_ < 8 ? WS_F + (unsigned)wg * 128 + ln_ * 16 : WS_BYTES, u32x4{1u, 1u, 1u, 1u});
    SBAR();
    op_step(F{}, T{}, F{}, wf[1], of[0], 0, wf[0], 0u, of[0]);
    op_step(T{}, T{}, F{}, wf[0], of[0], 1, wf[1], 0u, of[0]);
    int hd[8]; unsigned src[8];
#pragma unroll
    for (int m = 1; m < 8; ++m) { hd[m] = unit_head(m, h8, b); src[m] = o_in + (unsigned)hd[m] * 24576; asm volatile("" : "+s"(hd[m]), "+s"(src[m])); }
    unsigned rdy = 0;
    auto need = [&](int m) { if (!((rdy >> hd[m]) & 1)) rdy = (unsigned)spin_bit(lds + C_OFL, hd[m]); CFENCE(); };
#pragma unroll
    for (int m = 1; m <= 2; ++m) {
      need(m);
#pragma unroll
      for (int ks = 0; ks < 4; ++ks) of[m][ks] = ld16(rs, ln_ * 16 + ks * 1024, src[m]);
      SBAR();
      if (m == 1) STAMP(46);
    }
#pragma unroll
    for (int c = 1; c < 8; ++c) {
      if (c < 6) { need(c + 2); op_step(T{}, T{}, T{}, wf[c & 1], of[c], c + 1, wf[(c + 1) & 1], src[(c + 2) & 7], of[(c + 2) & 7]); }
      else if (c < 7) op_step(T{}, T{}, F{}, wf[c & 1], of[c], c + 1, wf[(c + 1) & 1], 0u, of[0]);
      else op_step(T{}, F{}, F{}, wf[1], of[7], 0, wf[0], 0u, of[0]);
    }
  }
  STAMP(49);
  {
    LAS char* xo = lds + EXTRA + widx * 4096 + ln_ * 16;
    LAS char* po = lds + EXTRA + (widx ^ 1) * 4096 + ln_ * 16;
#pragma unroll
    for (int r4 = 0; r4 < 4; ++r4) *(LAS f32x4*)(xo + r4 * 1024) = f32x4{acc[1][4 * r4], acc[1][4 * r4 + 1], acc[1][4 * r4 + 2], acc[1][4 * r4 + 3]};
    LGKM0();
    lds_post(lds, C_RED + 4 * wave, 1, ln_);
    spin_gt(lds + C_RED + 4 * partner, 0);
    const unsigned p_out = WS_P + (unsigned)((b * NH + (r32_ >> 3) * 8 + h8) * 24 + oct * 6 + widx) * 1024 + hh_ * 128 + (r32_ & 7) * 16;
    f32x4 t[4];
#pragma unroll
    for (int j = 0; j < 4; ++j) t[j] = *(LAS f32x4*)(po + j * 1024);
#pragma unroll
    for (int j = 0; j < 4; ++j) {
      const f32x4 v = {acc[0][4 * j] + t[j][0], acc[0][4 * j + 1] + t[j][1], acc[0][4 * j + 2] + t[j][2], acc[0][4 * j + 3] + t[j][3]};
      st16(rs, p_out + j * 256, __builtin_bit_cast(u32x4, v));
    }
    STAMP(50);
    VMW(0);
    st16(rs, ln_ < 8 ? WS_G + (unsigned)(wg * 8 + widx) * 128 + ln_ * 16 : WS_BYTES, u32x4{1u, 1u, 1u, 1u});
    VMW(0);
#pragma unroll 1
    for (int spin = 0; spin < SPIN_MAX; ++spin) {
      CFENCE();
      if (__all(ld4(rs, (ln_ & 3) * 8192 + (ln_ & 4) * 32, WS_G + (unsigned)((b * NH + h8) * 8 + 2 * rb) * 128) == 1u)) break;
    }
    CFENCE();
    STAMP(41);
    u32x4 pv[4];
#pragma unroll
    for (int j = 0; j < 4; ++j) pv[j] = ld16(rs, (ln_ >> 2) * 128 + (ln_ & 3) * 16, WS_P + (unsigned)(wg * 24 + j * 6 + 2 * rb) * 1024 + kh * 64);
    const f32x4 s = (__builtin_bit_cast(f32x4, pv[0]) + __builtin_bit_cast(f32x4, pv[1])) + (__builtin_bit_cast(f32x4, pv[2]) + __builtin_bit_cast(f32x4, pv[3]));
    *(u32x2*)(out + ((size_t)b * SQ + 32 * rb + 8 * oct + 4 * kh + (ln_ & 3)) * DM + 64 * h8 + 4 * (ln_ >> 2)) = u32x2{cvtpk(s[0], s[1]), cvtpk(s[2], s[3])};
  }
  STAMP(51);
}

extern "C" void kernel_launch(void* const* d_in, const int* in_sizes, int n_in, void* d_out, int out_size, void* d_ws, size_t ws_size,
                              hipStream_t stream) {
  (void)hipFuncSetAttribute((const void*)fused_kernel, hipFuncAttributeMaxDynamicSharedMemorySize, LDS_BYTES);
  hipLaunchKernelGGL(fused_kernel, dim3(256), dim3(512), LDS_BYTES, stream, (const __bf16*)d_in[0], (const __bf16*)d_in[1], (const __bf16*)d_in[2],
                     (const __bf16*)d_in[3], (const __bf16*)d_in[4], (__bf16*)d_out, (char*)d_ws);
}
```

```cpp
#include <hip/hip_runtime.h>
#include <hip/hip_bf16.h>
#include <stdint.h>
#include <type_traits>

#define LAS __attribute__((address_space(3)))
#define GAS __attribute__((address_space(1)))
#define DI __device__ __forceinline__
typedef __attribute__((ext_vector_type(4))) __bf16 bf16x4;
typedef __attribute__((ext_vector_type(8))) __bf16 bf16x8;
typedef __attribute__((ext_vector_type(2))) __bf16 bf16x2;
typedef __attribute__((ext_vector_type(4))) float f32x4;
typedef __attribute__((ext_vector_type(16))) float f32x16;
typedef __attribute__((ext_vector_type(2))) float f32x2;
typedef __attribute__((ext_vector_type(4))) unsigned u32x4;
typedef __attribute__((ext_vector_type(2))) unsigned u32x2;

constexpr int SQ = 96, DM = 512, NH = 32, SK = 1024, DH = 128;
constexpr int SLOT = 16384;
constexpr int KRING = 0, VRING = 4 * SLOT, EXTRA = 8 * SLOT;
constexpr int CTRL = EXTRA + 28672;
constexpr int C_WX = CTRL, C_KV = CTRL + 8, C_WL = CTRL + 16, C_WL2 = CTRL + 24, C_DONE = CTRL + 32, C_QRDY = CTRL + 64, C_MRG = CTRL + 96,
              C_RED = CTRL + 128, C_PEERS = CTRL + 192, C_OFL = CTRL + 200, C_OCNT = CTRL + 208, C_OISS = CTRL + 212,
              C_DUMMY = CTRL + 256;
constexpr int LDS_BYTES = 163840;
constexpr float SCL = 0.08838834764831845f * 1.4426950408889634f;
constexpr float PSUM_MAX = 4096.0f;
constexpr unsigned WBUF = 0x65840658u, XBUF = 0x73921739u;
constexpr unsigned OSLOT = 0x76210543u;
constexpr unsigned WS_O = 0, WS_P = 8u << 20, WS_H = 15u << 20, WS_F = WS_H + (64u << 10), WS_G = WS_F + (256u << 10), WS_BYTES = 16u << 20;
constexpr int SPIN_MAX = 1 << 16;
constexpr unsigned TAU = 56;

DI unsigned cvtpk(float lo, float hi) { f32x2 v = {lo, hi}; bf16x2 b = __builtin_convertvector(v, bf16x2); return __builtin_bit_cast(unsigned, b); }
DI f32x4 mfma16(bf16x8 a, bf16x8 b, f32x4 c) { return __builtin_amdgcn_mfma_f32_16x16x32_bf16(a, b, c, 0, 0, 0); }
DI f32x16 mfma32(bf16x8 a, bf16x8 b, f32x16 c) { return __builtin_amdgcn_mfma_f32_32x32x16_bf16(a, b, c, 0, 0, 0); }
DI bf16x4 trread(LAS char* p) { return __builtin_amdgcn_ds_read_tr16_b64_v4bf16((LAS bf16x4*)p); }
DI bf16x8 cat8(bf16x4 a, bf16x4 b) { return __builtin_shufflevector(a, b, 0, 1, 2, 3, 4, 5, 6, 7); }
DI bf16x8 asbf(const u32x4& w) { return __builtin_bit_cast(bf16x8, w); }
DI float xmax2(float v) {
  auto s = __builtin_amdgcn_permlane32_swap(__float_as_uint(v), __float_as_uint(v), false, false);
  return fmaxf(__uint_as_float(s[0]), __uint_as_float(s[1]));
}
DI u32x4 exp8(const f32x16& s, int o) {
  u32x4 w;
#pragma unroll
  for (int i = 0; i < 4; ++i) w[i] = cvtpk(__builtin_amdgcn_exp2f(s[o + 2 * i]), __builtin_amdgcn_exp2f(s[o + 2 * i + 1]));
  return w;
}
#define CFENCE() asm volatile("" ::: "memory")
#define LGKM0() do { CFENCE(); __builtin_amdgcn_s_waitcnt(0xc07f); CFENCE(); } while (0)
#define VMW(N) asm volatile("s_waitcnt vmcnt(" #N ")" ::: "memory")
#define SBAR() __builtin_amdgcn_sched_barrier(0)
#define PIN(x) asm volatile("" : "+v"(x))
DI int rfl(int v) { return __builtin_amdgcn_readfirstlane(v); }
DI int lds_word(LAS char* p) { return rfl(*(volatile LAS int*)p); }
DI int lds_min2(LAS char* p) { u32x2 v = *(volatile LAS u32x2*)p; return rfl(min((int)v[0], (int)v[1])); }
DI int lds_min6(LAS char* p) {
  u32x4 v = *(volatile LAS u32x4*)p; u32x2 w = *(volatile LAS u32x2*)(p + 16);
  return rfl(min(min(min((int)v[0], (int)v[1]), min((int)v[2], (int)v[3])), min((int)w[0], (int)w[1])));
}
DI LAS char* post_addr(LAS char* lds, int off, int lane) { return lds + (lane == 0 ? off : C_DUMMY + 4 * lane); }
DI void lds_post(LAS char* lds, int off, int v, int lane) { *(volatile LAS int*)post_addr(lds, off, lane) = v; }
DI void lds_bump(LAS char* p) { __hip_atomic_fetch_add((LAS int*)p, 1, __ATOMIC_RELAXED, __HIP_MEMORY_SCOPE_WORKGROUP); }
typedef __amdgpu_buffer_rsrc_t rsrc_t;
DI void st16(rsrc_t r, unsigned off, u32x4 v) { __builtin_amdgcn_raw_buffer_store_b128(v, r, (int)off, 0, 16); }
DI u32x4 ld16(rsrc_t r, unsigned voff, unsigned soff) { return __builtin_amdgcn_raw_buffer_load_b128(r, (int)voff, (int)soff, 16); }
DI void st4(rsrc_t r, unsigned off, unsigned v) { __builtin_amdgcn_raw_buffer_store_b32(v, r, (int)off, 0, 16); }
DI unsigned ld4(rsrc_t r, unsigned voff, unsigned soff) { return __builtin_amdgcn_raw_buffer_load_b32(r, (int)voff, (int)soff, 16); }
DI int fresh_lane() { int l; asm volatile("v_mbcnt_lo_u32_b32 %0, -1, 0\n\tv_mbcnt_hi_u32_b32 %0, -1, %0" : "=v"(l)); return l; }
DI int unit_head(int pos, int h8, int b) { int n = pos - 1 + 2 * (b & 3); n = n >= 7 ? n - 7 : n; return pos == 0 ? h8 : ((h8 + 1 + n) & 7); }

DI int spin_gt(LAS char* p, int n) {
  int v, r;
  asm volatile("1:\n\tds_read_b32 %1, %2\n\ts_waitcnt lgkmcnt(0)\n\tv_readfirstlane_b32 %0, %1\n\ts_cmp_le_i32 %0, %3\n\ts_cbranch_scc1 1b"
               : "=&s"(r), "=&v"(v) : "v"(p), "s"(n) : "memory", "scc");
  return r;
}
DI void spin_all_gt(LAS char* p, int n) {
  int v;
  asm volatile("1:\n\tds_read_b32 %0, %1\n\ts_waitcnt lgkmcnt(0)\n\tv_cmp_ge_i32 vcc, %2, %0\n\ts_cbranch_vccnz 1b"
               : "=&v"(v) : "v"(p), "v"(n) : "memory", "vcc");
}
DI int spin_bit(LAS char* p, int bit) {
  int v, r;
  asm volatile("1:\n\tds_read_b32 %1, %2\n\ts_waitcnt lgkmcnt(0)\n\tv_readfirstlane_b32 %0, %1\n\ts_bitcmp0_b32 %0, %3\n\ts_cbranch_scc1 1b"
               : "=&s"(r), "=&v"(v) : "v"(p), "s"(bit) : "memory", "scc");
  return r;
}
DI void wait_gt(int& have, int n, LAS char* p) {
  while (have <= n) { have = lds_word(p); if (have <= n) __builtin_amdgcn_s_sleep(1); }
  CFENCE();
}

struct Attn {
  f32x16 o[4];
  f32x16 negm;
  bf16x8 qf[8], kf[8];
  bf16x4 vf[16];
  u32x4 pf[2];
  bf16x8 ones;
  float lsum;
  int have;
  LAS char *kb[8], *vb[4], *land, *donep;

  DI void release() { LGKM0(); lds_bump(donep); }
  template <int S> DI void kread(int t) { kf[t] = *(LAS bf16x8*)(kb[t] + S * SLOT); }
  template <int S> DI void vread(int i) { vf[i] = trread(vb[(i >> 1) & 3] + S * SLOT + (i >> 3) * 4096 + (i & 1) * 2048); }
  DI bf16x8 vfrag(int m) { return cat8(vf[2 * m], vf[2 * m + 1]); }
  DI float rowmax(const f32x16& s) {
    float mx = s[0];
#pragma unroll
    for (int r = 1; r < 16; ++r) mx = fmaxf(mx, s[r]);
    return xmax2(mx);
  }
  DI f32x4 psum(const u32x4& p0, const u32x4& p1) { return mfma16(ones, asbf(p1), mfma16(ones, asbf(p0), f32x4{0.f, 0.f, 0.f, 0.f})); }
  template <int S> DI void tile(f32x16& cur, f32x16& nxt, int i) {
    release(); wait_gt(have, 2 * i + 2, land);
    SBAR();
    f32x4 ts = {0.f, 0.f, 0.f, 0.f};
    float ea, eb, pa = 0.f, pb = 0.f;
#pragma unroll
    for (int m = 0; m < 8; ++m) {
      nxt = mfma32(kf[m], qf[m], m == 0 ? negm : nxt);
      if (m < 5) { vread<S>(3 * m); vread<S>(3 * m + 1); vread<S>(3 * m + 2); }
      if (m == 5) vread<S>(15);
      ea = __builtin_amdgcn_exp2f(cur[2 * m]); eb = __builtin_amdgcn_exp2f(cur[2 * m + 1]);
      asm volatile("" : "+v"(ea), "+v"(eb));
      if (m > 0) { unsigned w = cvtpk(pa, pb); PIN(w); pf[(m - 1) >> 2][(m - 1) & 3] = w; }
      pa = ea; pb = eb;
      if (m == 6) ts = mfma16(ones, asbf(pf[0]), ts);
      SBAR();
    }
    pf[1][3] = cvtpk(pa, pb);
    ts = mfma16(ones, asbf(pf[1]), ts);
    release(); wait_gt(have, 2 * i + 3, land);
    if (__builtin_expect(__any(!(ts[0] <= PSUM_MAX)), 0)) {
      const float dl = fmaxf(rowmax(cur), 0.f);
      const float al = __builtin_amdgcn_exp2f(-dl);
#pragma unroll
      for (int r = 0; r < 16; ++r) { cur[r] -= dl; nxt[r] -= dl; negm[r] -= dl; }
      PIN(negm);
      pf[0] = exp8(cur, 0); pf[1] = exp8(cur, 8);
      ts = psum(pf[0], pf[1]);
      lsum *= al;
#pragma unroll
      for (int c = 0; c < 4; ++c) o[c] *= al;
      PIN(lsum); PIN(ts); PIN(nxt); PIN(pf[0]); PIN(pf[1]);
    }
    PIN(lsum); PIN(ts);
    lsum += ts[0];
    SBAR();
#pragma unroll
    for (int m = 0; m < 8; ++m) {
      o[m & 3] = mfma32(vfrag(m), asbf(pf[m >> 2]), o[m & 3]);
      if (m < 4) { kread<(S + 2) & 3>(2 * m); kread<(S + 2) & 3>(2 * m + 1); }
      SBAR();
    }
  }
};

__global__ __launch_bounds__(512, 2) void fused_kernel(const __bf16* __restrict__ x, const __bf16* __restrict__ kc, const __bf16* __restrict__ vc,
                                                       const __bf16* __restrict__ wq, const __bf16* __restrict__ wo, __bf16* __restrict__ out, char* ws) {
  extern __shared__ __attribute__((aligned(16))) char smem[];
  LAS char* lds = (LAS char*)smem;
  const int tid = threadIdx.x, lane = tid & 63, wave = (0x54321076u >> (4 * rfl(tid >> 6))) & 15;
  const int bid = blockIdx.x, xcd = bid & 7, idx = bid >> 3;
  const int oct = xcd & 3, h8 = idx & 7;
  const int h = oct * 8 + h8, b = (xcd >> 2) * 4 + (idx >> 3);
  const int wg = b * NH + h;
  using T = std::true_type; using F = std::false_type;

  if (__builtin_expect(wave >= 6, 1)) {
    __builtin_amdgcn_s_setprio(3);
    const int L = wave - 6;
    const int r16 = lane & 15, g = lane >> 4;
    unsigned oa[4], ob[4];
#pragma unroll
    for (int i = 0; i < 4; ++i) {
      oa[i] = i * (32768 - 1024) + g * 8192 + ((r16 ^ (g << 2)) << 4);
      const int row = 8 * i + (lane >> 3);
      ob[i] = row * 1024 - i * 1024 + (((lane & 7) ^ ((row >> 1) & 7)) << 4);
    }
    LAS char* lb = lds + L * 8192;
    int seen = 0;
#define GLDS(src, dst, off, aux) __builtin_amdgcn_global_load_lds((const GAS void*)(src), (LAS void*)(dst), 16, off, aux)
#define GLDS4(src, o0, o1, o2, o3, dst, aux) do { GLDS((src) + (o0), dst, 0, aux); GLDS((src) + (o1), dst, 1024, aux); GLDS((src) + (o2), dst, 2048, aux); GLDS((src) + (o3), dst, 3072, aux); } while (0)
#define POST(p, v) (*(volatile LAS int*)(p) = (v))
    auto wait_done = [&](int need) {
      while (seen < need) { seen = lds_min6(lds + C_DONE); if (seen < need) __builtin_amdgcn_s_sleep(1); }
      CFENCE();
    };
    LAS char* pw = post_addr(lds, C_WX + 4 * L, lane);
#pragma unroll
    for (int i = 0; i < 4; ++i) { PIN(oa[i]); PIN(ob[i]); }
    SBAR();
    const char* wp = (const char*)(wq + h * DH) + (size_t)L * 32 * 8192;
    const char* xp = (const char*)(x + ((size_t)b * SQ + 48 * L) * DM);
    {
      LAS char* xd = lds + (XBUF & 15) * SLOT + L * 6144;
      LAS char* wd = lb + (WBUF & 15) * SLOT;
      GLDS4(xp, ob[0], ob[1], ob[2], ob[3], xd, 0);
      GLDS(xp + 32768 + ob[0], xd + 4096, 0, 0); GLDS(xp + 32768 + ob[1], xd + 4096, 1024, 0);
      GLDS4(wp, oa[0], oa[1], oa[2], oa[3], wd, 0);
      GLDS4(wp + 16 * 8192, oa[0], oa[1], oa[2], oa[3], wd + 4096, 0);
    }
#pragma unroll 1
    for (int cc = 1; cc < 8; ++cc) {
      wait_done(cc - 4);
      LAS char* xd = lds + ((XBUF >> (4 * cc)) & 15) * SLOT + L * 6144;
      LAS char* wd = lb + ((WBUF >> (4 * cc)) & 15) * SLOT;
      const char* xs = xp + cc * 128;
      GLDS4(xs, ob[0], ob[1], ob[2], ob[3], xd, 0);
      GLDS(xs + 32768 + ob[0], xd + 4096, 0, 0); GLDS(xs + 32768 + ob[1], xd + 4096, 1024, 0);
      if (cc >= 2) { VMW(20); POST(pw, cc - 1); }
      const char* wsrc = wp + (size_t)cc * 64 * 8192;
      GLDS4(wsrc, oa[0], oa[1], oa[2], oa[3], wd, 0);
      GLDS4(wsrc + 16 * 8192, oa[0], oa[1], oa[2], oa[3], wd + 4096, 0);
      if (cc == 1) asm volatile("s_barrier" ::: "memory");
    }
    const char* kp = (const char*)(kc + (size_t)wg * SK * DH) + L * 8192;
    const char* vp = (const char*)(vc + (size_t)wg * SK * DH) + L * 8192;
    const unsigned ov = g * 256 + ((r16 ^ (g << 2)) << 4);
#pragma unroll
    for (int i = 0; i < 4; ++i) {
      oa[i] = g * 256 + ((r16 ^ (4 * i + g)) << 4);
      const int row = 8 * i + (lane >> 3);
      ob[i] = row * 1024 - i * 1024 + (((lane & 7) ^ (((row >> 1) & 1) << 2)) << 4);
    }
    auto issue_k = [&](int n) {
      const char* src = kp + n * SLOT; LAS char* dst = lb + KRING + (n & 3) * SLOT;
      GLDS4(src, oa[0], oa[1], oa[2], oa[3], dst, 2); GLDS4(src + 4096, oa[0], oa[1], oa[2], oa[3], dst + 4096, 2);
    };
    auto issue_v = [&](int n) {
      const char* src = vp + n * SLOT; LAS char* dst = lb + VRING + (n & 3) * SLOT;
      GLDS4(src, ov, ov, ov, ov, dst, 2); GLDS4(src + 4096, ov, ov, ov, ov, dst + 4096, 2);
    };
    wait_done(4);
    unsigned due = (unsigned)__builtin_amdgcn_s_memrealtime() + 2 * TAU;
    issue_k(0);
    VMW(22); POST(pw, 7);
    issue_k(1);
    VMW(16); POST(pw, 8);
    LAS char* pk = post_addr(lds, C_KV + 4 * L, lane);
    auto gate = [&](int j, int need) {
      if (seen < need) {
        seen = lds_min6(lds + C_DONE);
        if (seen < need) {
          VMW(0); POST(pk, j);
          do { __builtin_amdgcn_s_sleep(1); seen = lds_min6(lds + C_DONE); } while (seen < need);
        }
      }
      while ((int)((unsigned)__builtin_amdgcn_s_memrealtime() - due) < 0) __builtin_amdgcn_s_sleep(1);
      due += TAU;
      CFENCE();
    };
#define PUBLISH(j) do { VMW(8); POST(pk, j); } while (0)
#pragma unroll 1
    for (int n = 0; n < 12; ++n) {
      const int j = 2 * n + 2;
      gate(j, n < 4 ? ((0x9875 >> (4 * n)) & 15) : j + 1);
      issue_v(n); PUBLISH(j);
      gate(j + 1, n < 3 ? ((0x975 >> (4 * n)) & 15) : j + 2);
      issue_k(n + 2); PUBLISH(j + 1);
    }
    gate(26, 28); issue_k(14); PUBLISH(26);
    gate(27, 30); issue_k(15); PUBLISH(27);
    gate(28, 30); issue_v(12); PUBLISH(28);
    gate(29, 30); issue_v(13); PUBLISH(29);
    gate(30, 31); issue_v(14); PUBLISH(30);
    gate(31, 33); issue_v(15);
    PUBLISH(31);
    const char* op = (const char*)(wo + (size_t)(oct * 1024 + 64 * L) * DM + h8 * 64);
    auto issue_o = [&](int pos) {
      const char* src = op + (size_t)unit_head(pos, h8, b) * 128 * 1024; LAS char* dst = lb + ((OSLOT >> (4 * pos)) & 15) * SLOT;
      GLDS4(src, ob[0], ob[1], ob[2], ob[3], dst, 0); GLDS4(src + 32768, ob[0], ob[1], ob[2], ob[3], dst + 4096, 0);
    };
    LAS char* pl = post_addr(lds, C_WL + 4 * L, lane);
    __builtin_amdgcn_s_setprio(0);
    wait_done(36);
    issue_o(0);
    PUBLISH(32);
    wait_done(37); issue_o(1);
    while (lds_word(lds + C_OISS) < 6) __builtin_amdgcn_s_sleep(1);
    wait_done(41);
    issue_o(2);
    VMW(8); POST(pl, 2);
    if (L == 1) {
      LAS char* pl2 = post_addr(lds, C_WL2, lane);
#pragma unroll 1
      for (int pos = 3; pos < 8; ++pos) {
        issue_o(pos);
        op -= 65536; lb -= 8192; issue_o(pos); op += 65536; lb += 8192;
        VMW(16); POST(pos == 3 ? pl : pl2, pos);
      }
      VMW(0); POST(pl2, 8);
    } else {
      VMW(0); POST(pl, 3);
      const rsrc_t rs = __builtin_amdgcn_make_buffer_rsrc(ws, 0, WS_BYTES, 0x00020000);
      if (lds_word(lds + C_PEERS) == 0) {
        const bool peer = lane < 32 && ((lane >> 3) == oct || (lane & 7) == h8);
        const unsigned hello_to = peer ? WS_H + (unsigned)(b * NH + lane) * 128 + h * 4 : WS_BYTES;
        for (int spin = 0; spin < SPIN_MAX; ++spin) {
          st4(rs, hello_to, 1u); __builtin_amdgcn_s_sleep(8);
          if (!__any(peer && ld4(rs, (lane & 31) * 4, WS_H + (unsigned)wg * 128) != 1u)) break;
        }
        st4(rs, hello_to, 1u);
      }
      while (lds_word(lds + C_OCNT) < 6) __builtin_amdgcn_s_sleep(2);
      LAS char* pm = post_addr(lds, C_OFL, lane);
      for (int spin = 0; spin < SPIN_MAX; ++spin) {
        CFENCE();
        const unsigned m = (unsigned)__builtin_amdgcn_ballot_w64(ld4(rs, (lane & 7) * 128, WS_F + (unsigned)(b * NH + oct * 8) * 128) == 1u) & 0xffu;
        POST(pm, spin == SPIN_MAX - 1 ? 0xff : (int)m);
        if (m == 0xffu) break;
        __builtin_amdgcn_s_sleep(2);
      }
    }
    return;
  }
  const int kh = wave >= 3, rb = wave < 3 ? wave : (wave == 3 ? 2 : wave - 4);
  const int partner = rb == 2 ? (wave ^ 1) : (wave ^ 4);
  const int widx = 2 * rb + kh;
  if (wave == 0) *(LAS int*)(lds + CTRL + 4 * lane) = 0;
  asm volatile("s_waitcnt lgkmcnt(0)\n\ts_barrier" ::: "memory");
  const int r32 = lane & 31, hh = lane >> 5, blk = (lane >> 4) & 1, qq = (lane & 15) >> 2, p4 = lane & 3;
  const rsrc_t rs = __builtin_amdgcn_make_buffer_rsrc(ws, 0, WS_BYTES, 0x00020000);
  const unsigned o_out = WS_O + (unsigned)((wg * 3 + rb) * 8 + 4 * kh) * 1024;
  Attn st;
  st.donep = post_addr(lds, C_DONE + 4 * wave, lane);
  {
    f32x16 qa[2] = {f32x16{}, f32x16{}};
    const int wadr = (8 * hh + qq) * 256 + (((4 * kh + blk) ^ (qq << 1)) << 5) + p4 * 8;
    const int xrow = 32 * rb + r32;
    const int xadr = xrow * 128 + ((hh ^ ((xrow >> 1) & 7)) << 4);
    bf16x8 xa[4], xb[4];
    bf16x4 wa[16], wb[16];
    LAS char* wxp = lds + C_WX + 4 * (lane & 1);
    auto qp_step = [&](auto MMA, auto RD, bf16x8 (&xc)[4], bf16x4 (&wc)[16], int cn, bf16x8 (&xn)[4], bf16x4 (&wn)[16]) {
      LAS char* xb_ = lds + ((XBUF >> (4 * cn)) & 15) * SLOT;
      LAS char* wb_ = lds + ((WBUF >> (4 * cn)) & 15) * SLOT;
      int fl = 0;
#pragma unroll
      for (int m = 0; m < 8; ++m) {
        if constexpr (MMA.value) qa[m & 1] = mfma32(cat8(wc[2 * m], wc[2 * m + 1]), xc[m >> 1], qa[m & 1]);
        if constexpr (RD.value) {
          constexpr int lo[7] = {0, 4, 8, 11, 14, 17, 20};
          if (m < 6) {
#pragma unroll
            for (int i = lo[m]; i < lo[m + 1]; ++i) {
              const int ks = i / 5, e = i % 5 - 1;
              if (e < 0) xn[ks] = *(LAS bf16x8*)(xb_ + (xadr ^ (ks << 5)));
              else wn[4 * ks + e] = trread(wb_ + (wadr ^ ((e >> 1) << 6)) + ks * 4096 + (e & 1) * 1024);
            }
          }
          if (m == 5) fl = *(volatile LAS int*)wxp;
        }
        SBAR();
      }
      if constexpr (RD.value) {
        st.release();
        if (cn < 7 && __any(fl <= cn + 1)) spin_all_gt(wxp, cn + 1);
        CFENCE();
      }
    };
    while (lds_min2(lds + C_WX) < 1) __builtin_amdgcn_s_sleep(1);
    CFENCE();
    qp_step(F{}, T{}, xb, wb, 0, xa, wa);
#pragma unroll 1
    for (int cc = 0; cc < 6; cc += 2) {
      qp_step(T{}, T{}, xa, wa, cc + 1, xb, wb);
      qp_step(T{}, T{}, xb, wb, cc + 2, xa, wa);
    }
    qp_step(T{}, T{}, xa, wa, 7, xb, wb);
    qp_step(T{}, F{}, xb, wb, 0, xa, wa);
    spin_all_gt(lds + C_DONE + 4 * min(lane, 5), 5);
    LAS char* ql = lds + EXTRA + xrow * 256;
#pragma unroll
    for (int i = 0; i < 2; ++i)
#pragma unroll
      for (int r4 = 0; r4 < 4; ++r4) {
        const int ch = 4 * (2 * kh + i) + r4;
        u32x2 w = {cvtpk(qa[i][4 * r4] * SCL, qa[i][4 * r4 + 1] * SCL), cvtpk(qa[i][4 * r4 + 2] * SCL, qa[i][4 * r4 + 3] * SCL)};
        *(LAS u32x2*)(ql + ((ch ^ (r32 & 15)) << 4) + hh * 8) = w;
      }
    LGKM0();
    lds_post(lds, C_QRDY + 4 * wave, 1, lane);
    spin_all_gt(lds + (lane ? C_KV + 4 * kh : C_QRDY + 4 * partner), lane ? 1 : 0);
#pragma unroll
    for (int t = 0; t < 8; ++t) st.qf[t] = *(LAS bf16x8*)(ql + (((2 * t + hh) ^ (r32 & 15)) << 4));
  }
  {
    const int kadr = KRING + kh * 8192 + r32 * 256 + ((hh ^ (r32 & 15)) << 4);
    const int vadr = VRING + kh * 8192 + (4 * hh + qq) * 256 + ((blk ^ (qq << 1)) << 5) + p4 * 8;
#pragma unroll
    for (int t = 0; t < 8; ++t) st.kb[t] = lds + (kadr ^ (t << 5));
#pragma unroll
    for (int c = 0; c < 4; ++c) st.vb[c] = lds + (vadr ^ ((c ^ (2 * kh)) << 6));
    const unsigned one = (blk == (qq & 1)) ? 0x3f803f80u : 0u;
    st.ones = asbf(u32x4{one, one, one, one});
  }
  st.land = lds + C_KV + 4 * kh;
#pragma unroll
  for (int c = 0; c < 4; ++c) st.o[c] = f32x16{};
  st.lsum = 0.f; st.have = 0;
  f32x16 sA, sB;
  {
#pragma unroll
    for (int t = 0; t < 8; ++t) st.kread<0>(t);
    SBAR();
    sA = f32x16{};
#pragma unroll
    for (int t = 0; t < 8; ++t) sA = mfma32(st.kf[t], st.qf[t], sA);
    SBAR();
    st.release();
#pragma unroll
    for (int t = 0; t < 8; ++t) st.kread<1>(t);
    SBAR();
    const float mx = st.rowmax(sA);
#pragma unroll
    for (int r = 0; r < 16; ++r) { sA[r] -= mx; st.negm[r] = -mx; }
  }
  auto hello = [&]() {
    const int ln = fresh_lane();
    const bool peer = ln < 32 && ((ln >> 3) == oct || (ln & 7) == h8);
    st4(rs, peer ? WS_H + (unsigned)(b * NH + ln) * 128 + h * 4 : WS_BYTES, 1u);
  };
#pragma unroll 1
  for (int it = 0; it < 12; it += 4) {
    st.tile<0>(sA, sB, it); st.tile<1>(sB, sA, it + 1); st.tile<2>(sA, sB, it + 2); st.tile<3>(sB, sA, it + 3);
    if (wave == 2) {
      if (it == 0) {
        const int ln = fresh_lane();
        st16(rs, ln < 48 ? WS_G + (unsigned)wg * 1024 + ln * 16 : (ln < 56 ? WS_F : WS_H) + (unsigned)wg * 128 + (ln & 7) * 16, u32x4{0u, 0u, 0u, 0u});
      } else if (it == 4) {
        VMW(0);
        hello();
      }
    }
  }
  const int ln_ = fresh_lane();
  const int r32_ = ln_ & 31, hh_ = ln_ >> 5, blk_ = (ln_ >> 4) & 1, qq_ = (ln_ & 15) >> 2, p4_ = ln_ & 3;
  u32x4 of[8][4];
  {
    unsigned hin = 0;
    if (wave == 2) hin = ld4(rs, (ln_ & 31) * 4, WS_H + (unsigned)wg * 128);
    u32x4 pq[8];
    auto sm_step = [&](auto GG, f32x16& cur, f32x16& nxt) {
      constexpr int G = GG.value;
      if constexpr (G < 3) st.release();
      SBAR();
#pragma unroll
      for (int t = 0; t < 8; ++t) {
        if constexpr (G < 3) nxt = mfma32(st.kf[t], st.qf[t], t == 0 ? st.negm : nxt);
        unsigned w = cvtpk(__builtin_amdgcn_exp2f(cur[2 * t]), __builtin_amdgcn_exp2f(cur[2 * t + 1]));
        PIN(w); pq[2 * G + (t >> 2)][t & 3] = w;
        SBAR();
      }
      f32x4 ts = st.psum(pq[2 * G], pq[2 * G + 1]);
      if constexpr (G < 2) {
        wait_gt(st.have, 26 + G, st.land);
#pragma unroll
        for (int t = 0; t < 8; ++t) st.kread<2 + G>(t);
      }
      SBAR();
      if (__builtin_expect(__any(!(ts[0] <= PSUM_MAX)), 0)) {
        const float dl = __builtin_ceilf(fmaxf(st.rowmax(cur), 0.f));
        const float al = __builtin_amdgcn_exp2f(-dl);
#pragma unroll
        for (int r = 0; r < 16; ++r) { cur[r] -= dl; if (G < 3) nxt[r] -= dl; st.negm[r] -= dl; }
        PIN(st.negm);
        pq[2 * G] = exp8(cur, 0); pq[2 * G + 1] = exp8(cur, 8);
        ts = st.psum(pq[2 * G], pq[2 * G + 1]);
        st.lsum *= al;
#pragma unroll
        for (int c = 0; c < 4; ++c) st.o[c] *= al;
#pragma unroll
        for (int i = 0; i < 8 * G; ++i) { const unsigned w = pq[i >> 2][i & 3]; pq[i >> 2][i & 3] = cvtpk(__uint_as_float(w << 16) * al, __uint_as_float(w & 0xffff0000u) * al); }
        PIN(st.lsum); PIN(ts); if (G < 3) PIN(nxt);
#pragma unroll
        for (int i = 0; i < 2 * G + 2; ++i) PIN(pq[i]);
      }
      PIN(st.lsum); PIN(ts);
      st.lsum += ts[0];
    };
    sm_step(std::integral_constant<int, 0>{}, sA, sB);
    sm_step(std::integral_constant<int, 1>{}, sB, sA);
    if (wave == 2) {
      if (!__any(ln_ < 32 && ((ln_ >> 3) == oct || (ln_ & 7) == h8) && hin != 1u)) { hello(); lds_post(lds, C_PEERS, 1, ln_); }
    }
    sm_step(std::integral_constant<int, 2>{}, sA, sB);
    sm_step(std::integral_constant<int, 3>{}, sB, sA);
    const float lsum = st.lsum, mrun = -st.negm[0];
    {
      LAS char* xp_ = lds + EXTRA + widx * 4096 + ln_ * 16;
#pragma unroll
      for (int i = 0; i < 4; ++i) *(LAS u32x4*)(xp_ + i * 1024) = pq[4 + i];
      *(LAS f32x2*)(lds + EXTRA + 24576 + widx * 512 + ln_ * 8) = f32x2{mrun, lsum};
    }
    LGKM0();
    lds_post(lds, C_MRG + 4 * wave, 1, ln_);
    wait_gt(st.have, 28, st.land);
#pragma unroll
    for (int j = 0; j < 16; ++j) st.vread<0>(j);
    SBAR();
#pragma unroll
    for (int m = 0; m < 8; ++m) st.o[m & 3] = mfma32(st.vfrag(m), asbf(pq[m >> 2]), st.o[m & 3]);
    st.release();
    SBAR();
    LAS char* mrgp = lds + C_MRG + 4 * partner;
    spin_all_gt(ln_ < 6 ? lds + C_DONE + 4 * ln_ : mrgp, ln_ < 6 ? 34 : 0);
    u32x4 pp[4];
    const f32x2 ml = *(LAS f32x2*)(lds + EXTRA + 24576 + (widx ^ 1) * 512 + ln_ * 8);
    {
      LAS char* pp_ = lds + EXTRA + (widx ^ 1) * 4096 + ln_ * 16;
#pragma unroll
      for (int i = 0; i < 4; ++i) pp[i] = *(LAS u32x4*)(pp_ + i * 1024);
    }
    if (st.have <= 29) spin_gt(st.land, 29);
#pragma unroll
    for (int j = 0; j < 16; ++j) st.vread<1>(j ^ 4);
    const float mn = fmaxf(mrun, ml[0]);
    const float a0 = __builtin_amdgcn_exp2f(mrun - mn), a1 = __builtin_amdgcn_exp2f(ml[0] - mn);
    const float inv = 1.0f / (a0 * lsum + a1 * ml[1]);
    float f0 = a0 * inv, f1 = a1 * inv;
    PIN(f0); PIN(f1);
    SBAR();
    LAS char* xo = lds + KRING + widx * 8192 + ln_ * 16;
#pragma unroll
    for (int m = 0; m < 8; ++m) {
      const int mm = m < 2 ? m + 2 : (m < 4 ? m + 4 : (m < 6 ? m - 4 : m - 2));
      st.o[mm & 3] = mfma32(st.vfrag(mm), asbf(pq[2 + (mm >> 2)]), st.o[mm & 3]);
      if (m >= 5) {
#pragma unroll
        for (int q = (m - 5) * 3; q < (m - 5) * 3 + 3 && q < 8; ++q) {
          const f32x16& v = st.o[2 + (q >> 2)]; const int r = 4 * (q & 3);
          *(LAS f32x4*)(xo + q * 1024) = f32x4{v[r], v[r + 1], v[r + 2], v[r + 3]};
        }
      }
      SBAR();
    }
    st.release();
    lds_post(lds, C_MRG + 4 * wave, 2, ln_);
    st.vb[2] = st.vb[0] + (kh ? -8192 : 8192); st.vb[3] = st.vb[1] + (kh ? -8192 : 8192);
    LAS char* kvp = lds + C_KV + 4 * (ln_ & 1);
    LAS char* po = lds + KRING + (widx ^ 1) * 8192 + ln_ * 16;
    auto get_o = [&]() {
#pragma unroll
      for (int q = 0; q < 8; ++q) {
        const f32x4 t = *(volatile LAS f32x4*)(po + q * 1024);
#pragma unroll
        for (int r = 0; r < 4; ++r) st.o[2 + (q >> 2)][4 * (q & 3) + r] = t[r];
      }
    };
    spin_all_gt(kvp, 30);
#pragma unroll
    for (int j = 0; j < 16; ++j) st.vread<2>((j & 3) + ((j >> 2) & 1) * 8 + (j >> 3) * 4);
    const int fl = *(volatile LAS int*)mrgp;
    get_o();
    SBAR();
#pragma unroll
    for (int q = 0; q < 4; ++q) { const int m = (q & 1) + (q >> 1) * 4; st.o[m & 3] = mfma32(st.vfrag(m), asbf(pq[4 + (m >> 2)]), st.o[m & 3]); }
    SBAR();
    if (rfl(fl) < 2) { spin_gt(mrgp, 1); get_o(); }
    st.release(); lds_bump(st.donep);
#pragma unroll
    for (int q = 0; q < 4; ++q) { const int m = 2 + (q & 1) + (q >> 1) * 4; st.o[m & 3] = mfma32(st.vfrag(m), asbf(pp[m >> 2]), st.o[m & 3]); }
    SBAR();
    spin_all_gt(kvp, 31);
#pragma unroll
    for (int j = 0; j < 16; ++j) { const int mm = ((j >> 1) & 3) * 2 + (j >> 3); st.vread<3>(2 * mm + (j & 1)); }
    SBAR();
    auto fin = [&](int i, int s) {
      u32x4 w;
#pragma unroll
      for (int e = 0; e < 4; ++e) { const int r = 8 * s + 2 * e; w[e] = cvtpk(st.o[i][r] * f0 + st.o[2 + i][r] * f1, st.o[i][r + 1] * f0 + st.o[2 + i][r + 1] * f1); }
      of[0][2 * i + s] = w; st16(rs, o_out + ln_ * 16 + (2 * i + s) * 1024, w);
    };
#pragma unroll
    for (int q = 0; q < 8; ++q) {
      const int m = 2 * (q & 3) + (q >> 2);
      st.o[m & 3] = mfma32(st.vfrag(m), asbf((m & 2) ? pp[2 + (m >> 2)] : pq[6 + (m >> 2)]), st.o[m & 3]);
      if (q == 7) { st.release(); fin(0, 0); fin(0, 1); }
      SBAR();
    }
    fin(1, 0); fin(1, 1);
  }
  f32x16 acc[2] = {f32x16{}, f32x16{}};
  {
    const int tadr = kh * 8192 + (4 * hh_ + qq_) * 128 + (((qq_ >> 1) ^ kh) << 6) + blk_ * 32 + p4_ * 8;
    const unsigned o_in = WS_O + (unsigned)((b * NH + oct * 8) * 24 + rb * 8 + 4 * kh) * 1024;
    int whave = 0;
    bf16x4 wf[2][16];
    auto op_step = [&](auto MMA, auto RD, auto LD, bf16x4 (&wc)[16], u32x4 (&oc)[4], int pn, bf16x4 (&wn)[16], unsigned src, u32x4 (&on)[4]) {
      if constexpr (RD.value) { if (whave <= pn) whave = spin_gt(lds + (pn < 3 ? C_WL + 4 * kh : C_WL2), pn); }
      LAS char* wb_ = lds + ((OSLOT >> (4 * pn)) & 15) * SLOT;
#pragma unroll
      for (int m = 0; m < 8; ++m) {
        if constexpr (MMA.value) acc[m & 1] = mfma32(cat8(wc[2 * m], wc[2 * m + 1]), asbf(oc[m >> 1]), acc[m & 1]);
        if constexpr (RD.value) {
          if (m < 6) {
#pragma unroll
            for (int i = 3 * m; i < 3 * m + 3 && i < 16; ++i) wn[i] = trread(wb_ + (tadr ^ (((i >> 1) & 1) << 6)) + (i >> 2) * 2048 + (i & 1) * 1024);
          }
        }
        if constexpr (LD.value) { if (m & 1) on[m >> 1] = ld16(rs, ln_ * 16 + (m >> 1) * 1024, src); }
        SBAR();
      }
    };
    lds_bump(post_addr(lds, C_OISS, ln_));
    VMW(0);
    if (rfl(__hip_atomic_fetch_add((LAS int*)post_addr(lds, C_OCNT, ln_), 1, __ATOMIC_RELAXED, __HIP_MEMORY_SCOPE_WORKGROUP)) == 5)
      st16(rs, ln_ < 8 ? WS_F + (unsigned)wg * 128 + ln_ * 16 : WS_BYTES, u32x4{1u, 1u, 1u, 1u});
    SBAR();
    op_step(F{}, T{}, F{}, wf[1], of[0], 0, wf[0], 0u, of[0]);
    op_step(T{}, T{}, F{}, wf[0], of[0], 1, wf[1], 0u, of[0]);
    int hd[8]; unsigned src[8];
#pragma unroll
    for (int m = 1; m < 8; ++m) { hd[m] = unit_head(m, h8, b); src[m] = o_in + (unsigned)hd[m] * 24576; asm volatile("" : "+s"(hd[m]), "+s"(src[m])); }
    unsigned rdy = 0;
    auto need = [&](int m) { if (!((rdy >> hd[m]) & 1)) rdy = (unsigned)spin_bit(lds + C_OFL, hd[m]); CFENCE(); };
#pragma unroll
    for (int m = 1; m <= 2; ++m) {
      need(m);
#pragma unroll
      for (int ks = 0; ks < 4; ++ks) of[m][ks] = ld16(rs, ln_ * 16 + ks * 1024, src[m]);
      SBAR();
    }
#pragma unroll
    for (int c = 1; c < 8; ++c) {
      if (c < 6) { need(c + 2); op_step(T{}, T{}, T{}, wf[c & 1], of[c], c + 1, wf[(c + 1) & 1], src[(c + 2) & 7], of[(c + 2) & 7]); }
      else if (c < 7) op_step(T{}, T{}, F{}, wf[c & 1], of[c], c + 1, wf[(c + 1) & 1], 0u, of[0]);
      else op_step(T{}, F{}, F{}, wf[1], of[7], 0, wf[0], 0u, of[0]);
    }
  }
  {
    LAS char* xo = lds + EXTRA + widx * 4096 + ln_ * 16;
    LAS char* po = lds + EXTRA + (widx ^ 1) * 4096 + ln_ * 16;
#pragma unroll
    for (int r4 = 0; r4 < 4; ++r4) *(LAS f32x4*)(xo + r4 * 1024) = f32x4{acc[1][4 * r4], acc[1][4 * r4 + 1], acc[1][4 * r4 + 2], acc[1][4 * r4 + 3]};
    LGKM0();
    lds_post(lds, C_RED + 4 * wave, 1, ln_);
    spin_gt(lds + C_RED + 4 * partner, 0);
    const unsigned p_out = WS_P + (unsigned)((b * NH + (r32_ >> 3) * 8 + h8) * 24 + oct * 6 + widx) * 1024 + hh_ * 128 + (r32_ & 7) * 16;
    f32x4 t[4];
#pragma unroll
    for (int j = 0; j < 4; ++j) t[j] = *(LAS f32x4*)(po + j * 1024);
#pragma unroll
    for (int j = 0; j < 4; ++j) {
      const f32x4 v = {acc[0][4 * j] + t[j][0], acc[0][4 * j + 1] + t[j][1], acc[0][4 * j + 2] + t[j][2], acc[0][4 * j + 3] + t[j][3]};
      st16(rs, p_out + j * 256, __builtin_bit_cast(u32x4, v));
    }
    VMW(0);
    st16(rs, ln_ < 8 ? WS_G + (unsigned)(wg * 8 + widx) * 128 + ln_ * 16 : WS_BYTES, u32x4{1u, 1u, 1u, 1u});
    VMW(0);
#pragma unroll 1
    for (int spin = 0; spin < SPIN_MAX; ++spin) {
      CFENCE();
      if (__all(ld4(rs, (ln_ & 3) * 8192 + (ln_ & 4) * 32, WS_G + (unsigned)((b * NH + h8) * 8 + 2 * rb) * 128) == 1u)) break;
    }
    CFENCE();
    u32x4 pv[4];
#pragma unroll
    for (int j = 0; j < 4; ++j) pv[j] = ld16(rs, (ln_ >> 2) * 128 + (ln_ & 3) * 16, WS_P + (unsigned)(wg * 24 + j * 6 + 2 * rb) * 1024 + kh * 64);
    const f32x4 s = (__builtin_bit_cast(f32x4, pv[0]) + __builtin_bit_cast(f32x4, pv[1])) + (__builtin_bit_cast(f32x4, pv[2]) + __builtin_bit_cast(f32x4, pv[3]));
    *(u32x2*)(out + ((size_t)b * SQ + 32 * rb + 8 * oct + 4 * kh + (ln_ & 3)) * DM + 64 * h8 + 4 * (ln_ >> 2)) = u32x2{cvtpk(s[0], s[1]), cvtpk(s[2], s[3])};
  }
}

extern "C" void kernel_launch(void* const* d_in, const int* in_sizes, int n_in, void* d_out, int out_size, void* d_ws, size_t ws_size,
                              hipStream_t stream) {
  (void)hipFuncSetAttribute((const void*)fused_kernel, hipFuncAttributeMaxDynamicSharedMemorySize, LDS_BYTES);
  hipLaunchKernelGGL(fused_kernel, dim3(256), dim3(512), LDS_BYTES, stream, (const __bf16*)d_in[0], (const __bf16*)d_in[1], (const __bf16*)d_in[2],
                     (const __bf16*)d_in[3], (const __bf16*)d_in[4], (__bf16*)d_out, (char*)d_ws);
}
```
